```python
import torch

def _softmax(x, axis=-1):
    return torch.softmax(x, dim=axis)
def _gelu(x):
    return torch.nn.functional.gelu(x, approximate="tanh")
def _silu(x):
    return torch.nn.functional.silu(x)
def _sigmoid(x):
    return torch.sigmoid(x)
def _layernorm(x, axis=-1, eps=1e-5):
    m = x.mean(dim=axis, keepdim=True)
    v = x.var(dim=axis, keepdim=True, unbiased=False)
    return (x - m) / torch.sqrt(v + eps)
def _scatter_add(tgt, idx, src, axis=0):
    return tgt.clone().index_add_(axis, idx, src)
def _segment_sum(src, seg, n):
    out = torch.zeros((n,) + src.shape[1:], dtype=src.dtype)
    return out.index_add_(0, seg, src)
def _pad(x, pw):
    flat = tuple(v for pair in reversed(pw) for v in pair)
    return torch.nn.functional.pad(x, flat)

kernel_name = 'concat_heads__mha_block_cached__mha_block_cached__near_ridge'

def setup_inputs():

    _w = (lambda a: (lambda t: t.to(torch.bfloat16) if t.is_floating_point() else t)(torch.as_tensor(a)))
    def _A_setup():
        B, Sq, D = 8, 144, 512
        H, Sk, Dh = 32, 512, 128
        return {
            "x": _w((lambda *s: torch.randn(*s, dtype=torch.bfloat16) * 0.8)(B, Sq, D)),
            "k_cache": _w((lambda *s: torch.randn(*s, dtype=torch.bfloat16) * 0.8)(B, H, Sk, Dh)),
            "v_cache": _w((lambda *s: torch.randn(*s, dtype=torch.bfloat16) * 0.05)(B, H, Sk, Dh)),
            "wq": _w((lambda *s: torch.randn(*s, dtype=torch.bfloat16) * 0.8)(D, H * Dh) * (D ** -0.5)),
            "wo": _w((lambda *s: torch.randn(*s, dtype=torch.bfloat16) * 0.05)(H * Dh, D) * ((H * Dh) ** -0.5)),
        }
    _A_in = _A_setup()

    def _B_setup():
        B, Sq, D = 8, 144, 512
        H, Sk, Dh = 32, 512, 128
        return {
            "x": _w((lambda *s: torch.randn(*s, dtype=torch.bfloat16) * 0.8)(B, Sq, D)),
            "k_cache": _w((lambda *s: torch.randn(*s, dtype=torch.bfloat16) * 0.8)(B, H, Sk, Dh)),
            "v_cache": _w((lambda *s: torch.randn(*s, dtype=torch.bfloat16) * 0.05)(B, H, Sk, Dh)),
            "wq": _w((lambda *s: torch.randn(*s, dtype=torch.bfloat16) * 0.8)(D, H * Dh) * (D ** -0.5)),
            "wo": _w((lambda *s: torch.randn(*s, dtype=torch.bfloat16) * 0.05)(H * Dh, D) * ((H * Dh) ** -0.5)),
        }
    _B_in = _B_setup()

    return {
        "A_x": _A_in["x"],
        "A_k_cache": _A_in["k_cache"],
        "A_v_cache": _A_in["v_cache"],
        "A_wq": _A_in["wq"],
        "A_wo": _A_in["wo"],
        "B_x": _B_in["x"],
        "B_k_cache": _B_in["k_cache"],
        "B_v_cache": _B_in["v_cache"],
        "B_wq": _B_in["wq"],
        "B_wo": _B_in["wo"],
    }

def reference(A_x, A_k_cache, A_v_cache, A_wq, A_wo, B_x, B_k_cache, B_v_cache, B_wq, B_wo):
    def _A_ref(x, k_cache, v_cache, wq, wo):
        B, Sq, D = x.shape
        H, Dh = 32, 128
        q = (lambda x, *d: x.permute(*d))((x @ wq).reshape(B, Sq, H, Dh), 0, 2, 1, 3)
        scores = torch.einsum("bhqd,bhkd->bhqk", q, k_cache) * (Dh ** -0.5)
        p = _softmax(scores, axis=-1)
        o = torch.einsum("bhqk,bhkd->bhqd", p, v_cache)
        return (lambda x, *d: x.permute(*d))(o, 0, 2, 1, 3).reshape(B, Sq, H * Dh) @ wo

    def _B_ref(x, k_cache, v_cache, wq, wo):
        B, Sq, D = x.shape
        H, Dh = 32, 128
        q = (lambda x, *d: x.permute(*d))((x @ wq).reshape(B, Sq, H, Dh), 0, 2, 1, 3)
        scores = torch.einsum("bhqd,bhkd->bhqk", q, k_cache) * (Dh ** -0.5)
        p = _softmax(scores, axis=-1)
        o = torch.einsum("bhqk,bhkd->bhqd", p, v_cache)
        return (lambda x, *d: x.permute(*d))(o, 0, 2, 1, 3).reshape(B, Sq, H * Dh) @ wo

    a = _A_ref(A_x, A_k_cache, A_v_cache, A_wq, A_wo)
    b = _B_ref(B_x, B_k_cache, B_v_cache, B_wq, B_wo)
    return torch.concatenate([a, b], axis=-1)
```

```cpp
#include <hip/hip_runtime.h>
#include <hip/hip_bf16.h>
using bf16 = __hip_bfloat16;
constexpr int B=8, SQ=144, D=512, H=32, SK=512, DH=128;

__global__ void qproj(const bf16* x, const bf16* wq, float* q) {
  int n = blockIdx.x*256+threadIdx.x; int m = blockIdx.y;
  float acc=0; for(int k=0;k<D;++k) acc += __bfloat162float(x[m*D+k])*__bfloat162float(wq[(size_t)k*4096+n]);
  q[(size_t)m*4096+n] = __bfloat162float(__float2bfloat16(acc));
}
__global__ void attn(const float* q, const bf16* K, const bf16* V, float* o) {
  int qi = blockIdx.x, h = blockIdx.y, b = blockIdx.z; int t = threadIdx.x;
  __shared__ float s[SK]; __shared__ float qs[DH]; __shared__ float red[128];
  qs[t] = q[(size_t)(b*SQ+qi)*4096 + h*DH + t]; __syncthreads();
  const bf16* Kp = K + (size_t)(b*H+h)*SK*DH; const bf16* Vp = V + (size_t)(b*H+h)*SK*DH;
  float mx=-1e30f;
  for(int k=t;k<SK;k+=128){ float a=0; for(int d=0;d<DH;++d) a+=qs[d]*__bfloat162float(Kp[k*DH+d]); a*=0.08838834764831845f; s[k]=a; mx=fmaxf(mx,a);}
  red[t]=mx; __syncthreads(); for(int o2=64;o2>0;o2>>=1){ if(t<o2) red[t]=fmaxf(red[t],red[t+o2]); __syncthreads(); }
  mx=red[0]; __syncthreads();
  float sum=0; for(int k=t;k<SK;k+=128){ float e=__expf(s[k]-mx); s[k]=e; sum+=e; }
  red[t]=sum; __syncthreads(); for(int o2=64;o2>0;o2>>=1){ if(t<o2) red[t]+=red[t+o2]; __syncthreads(); }
  sum=red[0];
  float a=0; for(int k=0;k<SK;++k) a+=s[k]*__bfloat162float(Vp[k*DH+t]);
  o[(size_t)(b*SQ+qi)*4096 + h*DH + t] = __bfloat162float(__float2bfloat16(a/sum));
}
__global__ void oproj(const float* o, const bf16* wo, bf16* out, int off) {
  int n = blockIdx.x*256+threadIdx.x; int m = blockIdx.y;
  float acc=0; for(int k=0;k<4096;++k) acc += o[(size_t)m*4096+k]*__bfloat162float(wo[(size_t)k*512+n]);
  out[(size_t)m*1024+off+n] = __float2bfloat16(acc);
}
extern "C" void kernel_launch(void* const* d_in, const int* in_sizes, int n_in, void* d_out, int out_size, void* d_ws, size_t ws_size, hipStream_t stream) {
  float* q = (float*)d_ws; float* o = q + (size_t)B*SQ*4096;
  for (int blk=0;blk<2;++blk){
    const bf16* x=(const bf16*)d_in[blk*5+0]; const bf16* K=(const bf16*)d_in[blk*5+1]; const bf16* V=(const bf16*)d_in[blk*5+2];
    const bf16* wq=(const bf16*)d_in[blk*5+3]; const bf16* wo=(const bf16*)d_in[blk*5+4];
    qproj<<<dim3(16,B*SQ),256,0,stream>>>(x,wq,q);
    attn<<<dim3(SQ,H,B),128,0,stream>>>(q,K,V,o);
    oproj<<<dim3(2,B*SQ),256,0,stream>>>(o,wo,(bf16*)d_out,blk*512);
  }
}
```

```cpp
#include <hip/hip_runtime.h>
#include <hip/hip_bf16.h>
#include <type_traits>

typedef short bf16x8 __attribute__((ext_vector_type(8)));
typedef short bf16x4 __attribute__((ext_vector_type(4)));
typedef float f32x16 __attribute__((ext_vector_type(16)));
typedef float f32x4 __attribute__((ext_vector_type(4)));
typedef float f32x2 __attribute__((ext_vector_type(2)));
typedef __bf16 bfx2 __attribute__((ext_vector_type(2)));
typedef unsigned u32x4 __attribute__((ext_vector_type(4)));
typedef unsigned u32x2 __attribute__((ext_vector_type(2)));
#define LDS3 __attribute__((address_space(3)))
#define DI __device__ __forceinline__
typedef __amdgpu_buffer_rsrc_t rsrc_t;

constexpr int NB = 8, SQ = 144, DM = 512, NH = 32, SK = 512, DH = 128;
constexpr float CSC = 0.08838834764831845f * 1.4426950408889634f;
constexpr float THR = 6.0f;

constexpr unsigned L_RING = 0;
constexpr unsigned L_SX = 81920;
constexpr unsigned L_SW = 118784;
constexpr unsigned L_Q9 = 151552;
constexpr unsigned L_ST = 159744;
constexpr unsigned L_TAG = 160512;
constexpr unsigned L_QB0 = 49152;
constexpr unsigned L_PA = 81920;
constexpr unsigned L_PB = 114688;
constexpr unsigned L_QA = 0;
constexpr unsigned L_QC = 32768;
constexpr unsigned L_FIN = 0;
constexpr unsigned LDS_BYTES = 163840;
constexpr size_t WS_O = 0;
constexpr size_t WS_X = (size_t)2 * 8 * 144 * 8192;
constexpr size_t WS_F = WS_X + (size_t)64 * 16 * 18432;
constexpr size_t F_GEN = 0, F_START = 256, F_O = 4096, F_X = 16384;

DI unsigned cvtpk(float lo, float hi) { f32x2 v = {lo, hi}; bfx2 b = __builtin_convertvector(v, bfx2); return __builtin_bit_cast(unsigned, b); }
DI bf16x8 ld128(unsigned a) { return *(LDS3 bf16x8*)a; }
DI bf16x4 ldtr(unsigned a) { return __builtin_amdgcn_ds_read_tr16_b64_v4i16((LDS3 bf16x4*)a); }
DI bf16x8 cat(bf16x4 a, bf16x4 b) { return __builtin_shufflevector(a, b, 0, 1, 2, 3, 4, 5, 6, 7); }
DI f32x16 mfma32(bf16x8 a, bf16x8 b, f32x16 c) { return __builtin_amdgcn_mfma_f32_32x32x16_bf16(a, b, c, 0, 0, 0); }
DI f32x4 mfma16(bf16x8 a, bf16x8 b, f32x4 c) { return __builtin_amdgcn_mfma_f32_16x16x32_bf16(a, b, c, 0, 0, 0); }
DI float fmax3(float a, float b, float c) { return __builtin_elementwise_maximum(__builtin_elementwise_maximum(a, b), c); }
DI float fmax2(float a, float b) { return __builtin_elementwise_maximum(a, b); }
DI float ex2(float x) { return __builtin_amdgcn_exp2f(x); }
DI float sw32max(float x) { auto r = __builtin_amdgcn_permlane32_swap(__float_as_uint(x), __float_as_uint(x), false, false); return fmax2(__uint_as_float(r[0]), __uint_as_float(r[1])); }
DI float sw16max(float x) { auto r = __builtin_amdgcn_permlane16_swap(__float_as_uint(x), __float_as_uint(x), false, false); return fmax2(__uint_as_float(r[0]), __uint_as_float(r[1])); }
DI float sw32add(float x) { auto r = __builtin_amdgcn_permlane32_swap(__float_as_uint(x), __float_as_uint(x), false, false); return __uint_as_float(r[0]) + __uint_as_float(r[1]); }
DI float sw16add(float x) { auto r = __builtin_amdgcn_permlane16_swap(__float_as_uint(x), __float_as_uint(x), false, false); return __uint_as_float(r[0]) + __uint_as_float(r[1]); }

#define TV0()
#define TV1()
#define STAMP(i)
#define TBAR(acc) BARRIER()
#define BARRIER() asm volatile("s_waitcnt lgkmcnt(0)\n\ts_barrier" ::: "memory")
#define WAITV(n) asm volatile("s_waitcnt vmcnt(%0)" ::"i"(n) : "memory")
#define SB() __builtin_amdgcn_sched_barrier(0)
#define PIN(x) asm volatile("" : "+v"(x))
#define UNLIKELY(x) __builtin_expect(!!(x), 0)

template <int OFF, int POL> DI void dma(unsigned m0v, unsigned voff, rsrc_t r, unsigned soff) {
  if constexpr (POL == 1)
    asm volatile("s_mov_b32 m0, %0\n\ts_nop 0\n\tbuffer_load_dwordx4 %1, %2, %3 offen offset:%4 nt lds" ::"s"(m0v), "v"(voff), "s"(r), "s"(soff), "i"(OFF) : "memory");
  else if constexpr (POL == 2)
    asm volatile("s_mov_b32 m0, %0\n\ts_nop 0\n\tbuffer_load_dwordx4 %1, %2, %3 offen offset:%4 sc1 lds" ::"s"(m0v), "v"(voff), "s"(r), "s"(soff), "i"(OFF) : "memory");
  else
    asm volatile("s_mov_b32 m0, %0\n\ts_nop 0\n\tbuffer_load_dwordx4 %1, %2, %3 offen offset:%4 lds" ::"s"(m0v), "v"(voff), "s"(r), "s"(soff), "i"(OFF) : "memory");
}
template <bool ISK> DI void kv_issue4(unsigned slot_base, int wq, unsigned vkv, rsrc_t r, unsigned tile_off) {
  const unsigned m0v = slot_base + wq * 4096, so = tile_off + wq * 4096;
  if constexpr (ISK) { dma<0, 1>(m0v, vkv, r, so); dma<1024, 1>(m0v, vkv ^ 16u, r, so); dma<2048, 1>(m0v, vkv ^ 32u, r, so); dma<3072, 1>(m0v, vkv ^ 48u, r, so); }
  else { dma<0, 1>(m0v, vkv, r, so); dma<1024, 1>(m0v, vkv, r, so); dma<2048, 1>(m0v, vkv, r, so); dma<3072, 1>(m0v, vkv, r, so); }
}

template <bool ISV> DI void kv_h(unsigned sb, int hw, unsigned vkv, rsrc_t r, unsigned to) {
  kv_issue4<!ISV>(sb, 2 * hw, vkv, r, to); kv_issue4<!ISV>(sb, 2 * hw + 1, vkv, r, to);
}

struct Stg { rsrc_t r; unsigned vA, vB, m0, m0alt, rstride, so, sstride, pstride, last; };
struct GRd { unsigned xa, wa0, wa1; };
constexpr unsigned L_AX = 0, L_AW = 36864;
DI void stg_x(Stg& s, rsrc_t r, int lane, unsigned abase, unsigned arow) {
  const unsigned row16 = lane >> 2, sl = lane & 3;
  s.r = r; s.vA = s.vB = row16 * arow + (sl ^ ((0u - (row16 >> 2)) & 3)) * 16;
  s.m0 = L_SX; s.m0alt = L_AX; s.rstride = 9216; s.so = abase; s.sstride = 64; s.pstride = 16 * arow; s.last = 8;
}
DI void stg_w(Stg& s, rsrc_t r, int lane, unsigned wbase, unsigned wrow) {
  s.r = r; s.vA = ((lane >> 2) & 3) * wrow + (lane >> 4) * 64 + (lane & 3) * 16; s.vB = s.vA ^ 32u;
  s.m0 = L_SW; s.m0alt = L_AW; s.rstride = 8192; s.so = wbase; s.sstride = 32 * wrow; s.pstride = 4 * wrow; s.last = 0;
}
template <int POL> DI void stg_piece(const Stg& s, unsigned m0s, unsigned sos, int i) {
  if (i < 8) dma<0, POL>(m0s + i * 1024, (i & 2) ? s.vB : s.vA, s.r, sos + i * s.pstride);
  else dma<0, POL>(m0s + s.last * 1024, s.vA, s.r, sos + s.last * s.pstride);
}
template <int POL, int NP> DI void stg_part(const Stg& s, int st, int first, bool alt) {
  const unsigned m0s = (alt ? s.m0alt : s.m0) + (st & 3) * s.rstride + first * 1024, sos = s.so + st * s.sstride + first * s.pstride;
#pragma unroll
  for (int i = 0; i < NP; ++i) dma<0, POL>(m0s + i * 1024, (i & 2) ? s.vB : s.vA, s.r, sos + i * s.pstride);
}
template <int POL> DI void stg_stage(const Stg& s, int st, bool alt = false) {
  const unsigned m0s = (alt ? s.m0alt : s.m0) + (st & 3) * s.rstride, sos = s.so + st * s.sstride;
#pragma unroll
  for (int i = 0; i < 9; ++i) stg_piece<POL>(s, m0s, sos, i);
}
DI void grd_init(GRd& g, int w, int lane, unsigned xring, unsigned wring) {
  const unsigned g4 = lane >> 4;
  g.xa = xring + 64 * (lane & 15) + 16 * (g4 ^ ((0u - ((lane >> 2) & 3)) & 3));
  g.wa0 = wring + 2048 * g4 + 256 * w + 64 * ((lane >> 2) & 3) + 32 * (g4 & 1) + 8 * (lane & 3);
  g.wa1 = g.wa0 ^ 32u;
  PIN(g.xa); PIN(g.wa0); PIN(g.wa1);
}
DI bf16x8 g_xf(const GRd& g, int u, int m) { return ld128(g.xa + 9216 * u + 1024 * m); }
DI bf16x8 g_wf(const GRd& g, int u, int t) { const unsigned a = (t ? g.wa1 : g.wa0) + 8192 * u; return cat(ldtr(a), ldtr(a + 1024)); }

DI void gemm_step(const GRd& g, int u, f32x4 (&acc)[2][9]) {
  const bf16x8 w0 = g_wf(g, u, 0), w1 = g_wf(g, u, 1);
#pragma unroll
  for (int m = 0; m < 9; ++m) {
    const bf16x8 x = g_xf(g, u, m);
    acc[0][m] = mfma16(w0, x, acc[0][m]);
    acc[1][m] = mfma16(w1, x, acc[1][m]);
  }
}
template <bool LAST> DI void gemm_body(const GRd& g, const GRd& gn, f32x4 (&acc)[2][9], bf16x8 (&wf)[2], bf16x8 (&xf)[9]) {
#pragma unroll
  for (int u = 0; u < 4; ++u) {
#pragma unroll
    for (int m = 0; m < 5; ++m) {
      acc[0][m] = mfma16(wf[0], xf[m], acc[0][m]);
      if (m < 4) xf[5 + m] = g_xf(g, u, 5 + m);
      acc[1][m] = mfma16(wf[1], xf[m], acc[1][m]);
      SB();
    }
    const bool nb = !(LAST && u == 3);
    if (nb) BARRIER();
    SB();
    const GRd& q = u == 3 ? gn : g;
    bf16x8 wn[2];
#pragma unroll
    for (int m = 5; m < 9; ++m) {
      acc[0][m] = mfma16(wf[0], xf[m], acc[0][m]);
      if (nb) { if (m == 5) wn[0] = g_wf(q, (u + 1) & 3, 0); if (m == 6) wn[1] = g_wf(q, (u + 1) & 3, 1); if (m >= 7) xf[2 * (m - 7) + 1] = g_xf(q, (u + 1) & 3, 2 * (m - 7) + 1); }
      acc[1][m] = mfma16(wf[1], xf[m], acc[1][m]);
      if (nb) { if (m == 5) xf[0] = g_xf(q, (u + 1) & 3, 0); if (m >= 7) xf[2 * (m - 7) + 2] = g_xf(q, (u + 1) & 3, 2 * (m - 7) + 2); }
      SB();
    }
    if (nb) { wf[0] = wn[0]; wf[1] = wn[1]; }
  }
}
DI void gemm16(const GRd& ga, const GRd& gb, f32x4 (&acc)[2][9]) {
  bf16x8 wf[2], xf[9];
  wf[0] = g_wf(ga, 0, 0); wf[1] = g_wf(ga, 0, 1);
#pragma unroll
  for (int m = 0; m < 5; ++m) xf[m] = g_xf(ga, 0, m);
  SB();
  GRd c = ga, nx = gb;
#pragma unroll 1
  for (int it = 0; it < 3; ++it) { gemm_body<false>(c, nx, acc, wf, xf); const GRd t = c; c = nx; nx = t; }
  gemm_body<true>(c, nx, acc, wf, xf);
}
DI void acc_zero(f32x4 (&acc)[2][9]) {
#pragma unroll
  for (int t = 0; t < 2; ++t)
#pragma unroll
    for (int m = 0; m < 9; ++m) acc[t][m] = f32x4{};
}
DI void q_write(const f32x4 (&acc)[2][9], int w, int lane, unsigned qb, unsigned q9b) {
  const int q16 = lane & 15, g4 = lane >> 4;
#pragma unroll
  for (int t = 0; t < 2; ++t)
#pragma unroll
    for (int m = 0; m < 9; ++m) {
      const f32x4 a = acc[t][m] * CSC;
      u32x2 v = {cvtpk(a[0], a[1]), cvtpk(a[2], a[3])};
      if (m < 8) *(LDS3 u32x2*)(qb + (m >> 1) * 8192 + (2 * w + t) * 1024 + (16 * (m & 1) + q16 + 32 * (g4 >> 1)) * 16 + 8 * (g4 & 1)) = v;
      else *(LDS3 u32x2*)(q9b + w * 1024 + (q16 + 16 * (2 * t + (g4 >> 1))) * 16 + 8 * (g4 & 1)) = v;
    }
}

template <bool SECOND> DI void attention(int lane, int w, unsigned qaddr, f32x16 (&o)[4], float& l,
                                         rsrc_t rK, rsrc_t rV, unsigned ubase, unsigned ubase_next, unsigned vkv) {
  bf16x8 qf[8];
  if constexpr (!SECOND) {
#pragma unroll
    for (int i = 0; i < 8; ++i) qf[i] = ld128(qaddr + i * 1024);
  }
  const int h = lane >> 5;
  const unsigned rk = lane & 31;
  const unsigned ka0 = 1024 * (rk >> 2) + 64 * (rk & 3) + 16 * (h ^ ((rk >> 2) & 3));
  const unsigned va = 1024 * h + 64 * ((lane >> 2) & 3) + 32 * ((lane >> 4) & 1) + 8 * (lane & 3);
#pragma unroll
  for (int c = 0; c < 4; ++c) o[c] = f32x16{};
  l = 0.f;
  f32x16 negm = {};
  float thr = -3e38f;
  const unsigned vk1 = vkv ^ 16u, vk2 = vkv ^ 32u, vk3 = vkv ^ 48u;
  unsigned long long wA = 0, wB = 0, wV = 0;
  int slot = SECOND ? 1 : 0;
  bf16x8 kf[16];
  unsigned kA, kB;
#define KLD(i) ld128((((i) & 1) ? kB : kA) + 8192 * ((i) >> 3) + 256 * (((i) & 7) >> 1))
#define VLOAD(j) cat(ldtr(vA + 4096 * ((j) >> 2) + 256 * ((j) & 3)), ldtr(vA + 4096 * ((j) >> 2) + 2048 + 256 * ((j) & 3)))
#define SREG(p, e) ((p) < 8 ? s0[2 * (p) + (e)] : s1[2 * ((p) - 8) + (e)])
#define EX(p) do { if ((p) < 8) { s0[2 * (p)] = ex2(s0[2 * (p)]); s0[2 * (p) + 1] = ex2(s0[2 * (p) + 1]); PIN(s0[2 * (p)]); PIN(s0[2 * (p) + 1]); } \
                   else { s1[2 * ((p) - 8)] = ex2(s1[2 * ((p) - 8)]); s1[2 * ((p) - 8) + 1] = ex2(s1[2 * ((p) - 8) + 1]); PIN(s1[2 * ((p) - 8)]); PIN(s1[2 * ((p) - 8) + 1]); } } while (0)
#define FIN(p) do { pw[(p) >> 2][(p) & 3] = cvtpk(SREG(p, 0), SREG(p, 1)); PIN(pw[(p) >> 2][(p) & 3]); \
                    if ((p) < 8) { ps0 += SREG(p, 0); ps0 += SREG(p, 1); PIN(ps0); } else { ps1 += SREG(p, 0); ps1 += SREG(p, 1); PIN(ps1); } } while (0)
  BARRIER();
  if constexpr (SECOND) {
#pragma unroll
    for (int i = 0; i < 8; ++i) qf[i] = ld128(qaddr + i * 1024);
  }
  { kA = slot * 16384 + ka0; PIN(kA); kB = kA ^ 32u; PIN(kB); slot = slot == 4 ? 0 : slot + 1; }
#pragma unroll
  for (int i = 0; i < 4; ++i) kf[i] = KLD(i);
  SB();
  unsigned isl = SECOND ? 4 * 16384 : 3 * 16384;
  for (int t = 0; t < 8; ++t) {
    const unsigned vso = (t < 7 ? ubase + (t + 1) * 16384 : ubase_next) + w * 4096;
    const unsigned kso = (t < 6 ? ubase + (t + 2) * 16384 : ubase_next + (t - 6) * 16384) + w * 4096;
    const unsigned vm0 = isl + w * 4096; isl = isl == 4 * 16384 ? 0 : isl + 16384;
    const unsigned km0 = isl + w * 4096; isl = isl == 4 * 16384 ? 0 : isl + 16384;
    const bool dov = !SECOND || t < 7, dok = !SECOND || t < 6;
    f32x16 s0, s1;
    u32x4 pw[4];
    bf16x8 vf[16];
    float mx = 0.f, ps0 = 0.f, ps1 = 0.f, adj1 = 0.f, al1 = 1.f;
    bool f0 = false, f1 = false;
    unsigned vA = 0;
#pragma unroll
    for (int i = 0; i < 16; ++i) {
      if (i + 4 < 16) kf[i + 4] = KLD(i + 4);
      if (i == 0) s0 = mfma32(kf[0], qf[0], negm);
      else if (i < 8) s0 = mfma32(kf[i], qf[i], s0);
      else if (i == 8) s1 = mfma32(kf[8], qf[0], negm);
      else s1 = mfma32(kf[i], qf[i & 7], s1);
      if (i >= 1 && i <= 8) {
        SB();
        if (i <= 4) { if (dov) { if (i == 1) dma<0, 1>(vm0, vkv, rV, vso); else if (i == 2) dma<1024, 1>(vm0, vkv, rV, vso); else if (i == 3) dma<2048, 1>(vm0, vkv, rV, vso); else dma<3072, 1>(vm0, vkv, rV, vso); } }
        else if (dok) { if (i == 5) dma<0, 1>(km0, vkv, rK, kso); else if (i == 6) dma<1024, 1>(km0, vk1, rK, kso); else if (i == 7) dma<2048, 1>(km0, vk2, rK, kso); else dma<3072, 1>(km0, vk3, rK, kso); }
      }
      if (i == 9) { mx = fmax3(s0[0], s0[1], s0[2]); mx = fmax3(mx, s0[3], s0[4]); mx = fmax3(mx, s0[5], s0[6]); mx = fmax3(mx, s0[7], s0[8]); PIN(mx); }
      if (i == 10) {
        mx = fmax3(mx, s0[9], s0[10]); mx = fmax3(mx, s0[11], s0[12]); mx = fmax3(mx, s0[13], s0[14]); mx = fmax2(mx, s0[15]);
        if (UNLIKELY(__any(mx > thr))) {
          mx = sw32max(mx);
          const bool first = thr < 0.f;
          const float d = first ? mx : fmax2(mx, 0.f), al = first ? 1.f : ex2(-d);
          thr = THR;
#pragma unroll
          for (int r = 0; r < 16; ++r) { s0[r] -= d; negm[r] -= d; }
#pragma unroll
          for (int c = 0; c < 4; ++c)
#pragma unroll
            for (int r = 0; r < 16; ++r) o[c][r] *= al;
          l *= al; adj1 = d; f0 = true;
        }
      }
      if (i >= 11) EX(i - 11);
      if (i >= 12) FIN(i - 12);
      if (i == 13) {
        TV0();
        if constexpr (SECOND) { if (UNLIKELY(t >= 6)) { if (t == 6) WAITV(8); else WAITV(0); } else WAITV(12); } else WAITV(12);
        TV1();
        TBAR(wB);
        vA = slot * 16384 + va; PIN(vA); slot = slot == 4 ? 0 : slot + 1;
        vf[0] = VLOAD(0);
      }
      if (i == 14) { vf[1] = VLOAD(1); vf[2] = VLOAD(2); }
      if (i == 15) vf[3] = VLOAD(3);
      SB();
    }
#pragma unroll
    for (int j = 0; j < 16; ++j) {
      const int sp = j >> 2, c = j & 3;
      if (j + 4 < 16) vf[j + 4] = VLOAD(j + 4);
      o[c] = mfma32(vf[j], __builtin_bit_cast(bf16x8, pw[sp]), o[c]);
      if (j < 3) EX(5 + j);
      if (j < 4) FIN(4 + j);
      if (j == 1) {
        if (UNLIKELY(f0)) {
#pragma unroll
          for (int r = 0; r < 16; ++r) s1[r] -= adj1;
        }
        mx = fmax3(s1[0], s1[1], s1[2]); mx = fmax3(mx, s1[3], s1[4]); mx = fmax3(mx, s1[5], s1[6]); mx = fmax3(mx, s1[7], s1[8]); PIN(mx);
      }
      if (j == 2) {
        mx = fmax3(mx, s1[9], s1[10]); mx = fmax3(mx, s1[11], s1[12]); mx = fmax3(mx, s1[13], s1[14]); mx = fmax2(mx, s1[15]);
        if (UNLIKELY(__any(mx > THR))) {
          mx = sw32max(mx);
          const float d = fmax2(mx, 0.f);
          al1 = ex2(-d);
#pragma unroll
          for (int r = 0; r < 16; ++r) { s1[r] -= d; negm[r] -= d; }
          f1 = true;
        }
      }
      if (j >= 3 && j < 11) EX(8 + j - 3);
      if (j >= 4 && j < 12) FIN(8 + j - 4);
      if (j == 7) {
        l += ps0;
        if (UNLIKELY(f1)) {
#pragma unroll
          for (int cc = 0; cc < 4; ++cc)
#pragma unroll
            for (int r = 0; r < 16; ++r) o[cc][r] *= al1;
          l *= al1;
        }
      }
      if (j == 13) {
        TV0();
        if constexpr (SECOND) { if (UNLIKELY(t >= 6)) { if (t == 6) WAITV(4); } else WAITV(8); } else WAITV(8);
        TV1();
        TBAR(wA);
        kA = slot * 16384 + ka0; PIN(kA); kB = kA ^ 32u; PIN(kB); slot = slot == 4 ? 0 : slot + 1;
        kf[0] = KLD(0); kf[1] = KLD(1);
      }
      if (j == 14) { kf[2] = KLD(2); kf[3] = KLD(3); }
      SB();
    }
    l += ps1;
  }
#undef KLD
#undef VLOAD
#undef EX
#undef FIN
#undef SREG
}

struct Ninth { f32x4 o9[8]; float m9, l9; bf16x8 q9f[4]; bf16x8 p9; bf16x8 nf[8]; unsigned k9a, v9a; };
DI void ninth_init(Ninth& n, int w, int lane, unsigned q9base) {
  const int g4 = lane >> 4;
  const unsigned r9 = 32 * (w & 1) + (lane & 15);
  n.k9a = 1024 * (r9 >> 2) + 64 * (r9 & 3) + 16 * (g4 ^ ((r9 >> 2) & 3));
  n.v9a = 1024 * (8 * (w & 1) + g4) + 64 * ((lane >> 2) & 3) + 8 * (lane & 3);
#pragma unroll
  for (int s = 0; s < 4; ++s) n.q9f[s] = ld128(q9base + lane * 16 + 1024 * s);
#pragma unroll
  for (int c = 0; c < 8; ++c) n.o9[c] = f32x4{};
  n.m9 = -1e30f; n.l9 = 0.f; n.p9 = bf16x8{};
#pragma unroll
  for (int c = 0; c < 8; ++c) n.nf[c] = bf16x8{};
}
DI void ninth_qk(Ninth& n, unsigned kb) {
  f32x4 sa = {}, sb = {};
#pragma unroll
  for (int s = 0; s < 4; ++s) { sa = mfma16(ld128(kb + n.k9a + 256 * s), n.q9f[s], sa); sb = mfma16(ld128(kb + n.k9a + 4096 + 256 * s), n.q9f[s], sb); }
  float mx9 = fmax3(fmax3(sa[0], sa[1], sa[2]), sa[3], fmax3(fmax3(sb[0], sb[1], sb[2]), sb[3], sb[3]));
  if (UNLIKELY(__any(mx9 > n.m9 + THR))) {
    mx9 = sw32max(sw16max(mx9));
    const float mn = fmax2(n.m9, mx9), al = ex2(n.m9 - mn);
    n.m9 = mn; n.l9 *= al;
#pragma unroll
    for (int c = 0; c < 8; ++c)
#pragma unroll
      for (int r = 0; r < 4; ++r) n.o9[c][r] *= al;
  }
#pragma unroll
  for (int r = 0; r < 4; ++r) { sa[r] = ex2(sa[r] - n.m9); sb[r] = ex2(sb[r] - n.m9); }
  n.l9 += ((sa[0] + sa[1]) + (sa[2] + sa[3])) + ((sb[0] + sb[1]) + (sb[2] + sb[3]));
  u32x4 p = {cvtpk(sa[0], sa[1]), cvtpk(sa[2], sa[3]), cvtpk(sb[0], sb[1]), cvtpk(sb[2], sb[3])};
  n.p9 = __builtin_bit_cast(bf16x8, p);
}
DI void ninth_pv(Ninth& n, unsigned vb) {
#pragma unroll
  for (int c = 0; c < 8; ++c) {
    const unsigned a = vb + n.v9a + 256 * (c >> 1) + 32 * (c & 1);
    n.o9[c] = mfma16(cat(ldtr(a), ldtr(a + 4096)), n.p9, n.o9[c]);
  }
}
struct Duty { Stg x, w; };
template <int POL, bool NIN, bool PVP, int NX> DI void hintA(Ninth& n, unsigned cs, const Duty& d, int st) {
  auto stage = [&](int s) {
    if constexpr (NX >= 0) {
      const unsigned xm = d.x.m0 + (s & 3) * 9216, xs = d.x.so + s * 64, wm = d.w.m0 + (s & 3) * 8192, wsf = d.w.so + s * d.w.sstride;
      dma<0, POL>(wm, d.w.vA, d.w.r, wsf); dma<0, POL>(wm + 1024, d.w.vA, d.w.r, wsf + d.w.pstride);
#pragma unroll
      for (int i = 0; i < NX; ++i) dma<0, POL>(xm + i * 1024, d.x.vA, d.x.r, xs + i * d.x.pstride);
    }
  };
  if constexpr (NIN) {
    unsigned na = cs + n.k9a;
    PIN(na);
#pragma unroll
    for (int s = 0; s < 4; ++s) { n.nf[2 * s] = ld128(na + 256 * s); n.nf[2 * s + 1] = ld128(na + 4096 + 256 * s); }
  }
  SB();
  if constexpr (PVP) {
#pragma unroll
    for (int c = 0; c < 4; ++c) n.o9[c] = mfma16(n.nf[c], n.p9, n.o9[c]);
  }
  SB();
  stage(st);
  SB();
  if constexpr (PVP) {
#pragma unroll
    for (int c = 4; c < 8; ++c) n.o9[c] = mfma16(n.nf[c], n.p9, n.o9[c]);
  }
  SB();
  stage(st + 1);
}
template <bool NIN, bool GM, int U> DI void hintB(Ninth& n, unsigned cs, const GRd& g, f32x4 (&acc)[2][9]) {
  bf16x8 wf[4], xf[18];
  f32x4 sa = {}, sb = {};
  unsigned na = cs + n.v9a;
  PIN(na);
  if constexpr (GM) { wf[0] = g_wf(g, U, 0); xf[0] = g_xf(g, U, 0); wf[1] = g_wf(g, U, 1); xf[1] = g_xf(g, U, 1); xf[2] = g_xf(g, U, 2); xf[3] = g_xf(g, U, 3); }
  SB();
#pragma unroll
  for (int m = 0; m < 18; ++m) {
    if constexpr (NIN) {
      if (m < 4) { sa = mfma16(n.nf[2 * m], n.q9f[m], sa); sb = mfma16(n.nf[2 * m + 1], n.q9f[m], sb); }
    }
    if constexpr (GM) {
      const int hs = m / 9, mm = m % 9;
      acc[0][mm] = mfma16(wf[2 * hs], xf[m], acc[0][mm]);
      if (m + 4 < 18) xf[m + 4] = g_xf(g, U + (m + 4) / 9, (m + 4) % 9);
      acc[1][mm] = mfma16(wf[2 * hs + 1], xf[m], acc[1][mm]);
      if (m == 3) wf[2] = g_wf(g, U + 1, 0);
      if (m == 4) wf[3] = g_wf(g, U + 1, 1);
    }
    if constexpr (NIN) {
      if (m >= 5 && m < 13) { const int c = m - 5; const unsigned a = na + 256 * (c >> 1) + 32 * (c & 1); n.nf[c] = cat(ldtr(a), ldtr(a + 4096)); }
      if (m == 6) {
        float mx9 = fmax3(fmax3(sa[0], sa[1], sa[2]), sa[3], fmax3(fmax3(sb[0], sb[1], sb[2]), sb[3], sb[3]));
        if (UNLIKELY(__any(mx9 > n.m9 + THR))) {
          mx9 = sw32max(sw16max(mx9));
          const float mn = fmax2(n.m9, mx9), al = ex2(n.m9 - mn);
          n.m9 = mn; n.l9 *= al;
#pragma unroll
          for (int c = 0; c < 8; ++c)
#pragma unroll
            for (int r = 0; r < 4; ++r) n.o9[c][r] *= al;
        }
      }
      if (m == 7) {
#pragma unroll
        for (int r = 0; r < 4; ++r) { sa[r] = ex2(sa[r] - n.m9); PIN(sa[r]); }
      }
      if (m == 8) {
#pragma unroll
        for (int r = 0; r < 4; ++r) { sb[r] = ex2(sb[r] - n.m9); PIN(sb[r]); }
      }
      if (m == 9) {
        n.l9 += ((sa[0] + sa[1]) + (sa[2] + sa[3])) + ((sb[0] + sb[1]) + (sb[2] + sb[3]));
        u32x4 p = {cvtpk(sa[0], sa[1]), cvtpk(sa[2], sa[3]), cvtpk(sb[0], sb[1]), cvtpk(sb[2], sb[3])};
        n.p9 = __builtin_bit_cast(bf16x8, p);
        PIN(n.p9); PIN(n.l9);
      }
    }
    SB();
  }
}
DI void ninth_flush(Ninth& n) {
#pragma unroll
  for (int c = 0; c < 8; ++c) n.o9[c] = mfma16(n.nf[c], n.p9, n.o9[c]);
}
DI void ninth_dump(Ninth& n, int w, int lane, unsigned reg) {
  const int g4 = lane >> 4;
  const float l9s = sw32add(sw16add(n.l9));
  const unsigned p9 = reg + w * 8192 + (lane & 15) * 512;
#pragma unroll
  for (int c = 0; c < 8; ++c) *(LDS3 f32x4*)(p9 + 16 * ((4 * c + g4) ^ (lane & 15))) = n.o9[c];
  *(LDS3 f32x2*)(L_ST + (w * 16 + (lane & 15)) * 8) = f32x2{n.m9, l9s};
}

DI void epilogue1(int w, int lane, f32x16 (&o)[4], float l, rsrc_t rO, unsigned ounit, unsigned reg) {
  const int h = lane >> 5, q = lane & 31, g4 = lane >> 4;
  const float rl = 1.0f / sw32add(l);
  const unsigned st = reg + w * 8192;
#pragma unroll
  for (int c = 0; c < 4; ++c)
#pragma unroll
    for (int i = 0; i < 4; ++i) {
      u32x2 v = {cvtpk(o[c][4 * i] * rl, o[c][4 * i + 1] * rl), cvtpk(o[c][4 * i + 2] * rl, o[c][4 * i + 3] * rl)};
      *(LDS3 u32x2*)(st + q * 256 + 16 * ((4 * c + i) ^ (q & 15)) + 8 * h) = v;
    }
  asm volatile("s_waitcnt lgkmcnt(0)" ::: "memory");
#pragma unroll
  for (int j = 0; j < 8; ++j) {
    const unsigned row = 4 * j + g4;
    const u32x4 v = *(LDS3 u32x4*)(st + row * 256 + 16 * ((lane & 15) ^ (row & 15)));
    __builtin_amdgcn_raw_buffer_store_b128(v, rO, ounit + (32 * w + row) * 8192 + (lane & 15) * 16, 0, 16);
  }
}
DI void epilogue2(int w, int lane, rsrc_t rO, unsigned ounit, unsigned reg) {
  const int tid = w * 64 + lane, q = tid >> 4, dc = tid & 15;
  float mm[4], ll[4];
#pragma unroll
  for (int i = 0; i < 4; ++i) { mm[i] = *(LDS3 float*)(L_ST + (i * 16 + q) * 8); ll[i] = *(LDS3 float*)(L_ST + (i * 16 + q) * 8 + 4); }
  const float M = fmax2(fmax2(mm[0], mm[1]), fmax2(mm[2], mm[3]));
  float L = 0.f, sc[4];
#pragma unroll
  for (int i = 0; i < 4; ++i) { sc[i] = ex2(mm[i] - M); L += ll[i] * sc[i]; }
  const float rl = 1.0f / L;
  f32x4 a = {}, b = {};
#pragma unroll
  for (int i = 0; i < 4; ++i) {
    const unsigned p = reg + i * 8192 + q * 512;
    const f32x4 x = *(LDS3 f32x4*)(p + 16 * ((2 * dc) ^ q)), y = *(LDS3 f32x4*)(p + 16 * ((2 * dc + 1) ^ q));
    a += x * (sc[i] * rl); b += y * (sc[i] * rl);
  }
  u32x4 v = {cvtpk(a[0], a[1]), cvtpk(a[2], a[3]), cvtpk(b[0], b[1]), cvtpk(b[2], b[3])};
  __builtin_amdgcn_raw_buffer_store_b128(v, rO, ounit + (128 + q) * 8192 + dc * 16, 0, 16);
}

typedef unsigned long long u64;
DI u64 ld_flag(const u64* p) { return __hip_atomic_load(p, __ATOMIC_RELAXED, __HIP_MEMORY_SCOPE_AGENT); }
DI void st_flag(u64* p, u64 v) { __hip_atomic_store(p, v, __ATOMIC_RELAXED, __HIP_MEMORY_SCOPE_AGENT); }
DI void poll_flags(const u64* p, int n, int skip, u64 tag, int lane) {
  const bool act = lane < n && lane != skip;
  for (int it = 0; it < (1 << 20); ++it) {
    const u64 v = act ? ld_flag(p + lane) : tag;
    if (__all(v == tag)) break;
    __builtin_amdgcn_s_sleep(2);
  }
  asm volatile("" ::: "memory");
}
DI u64 get_tag() {
  const u32x2 t = *(LDS3 u32x2*)L_TAG;
  return ((u64)(unsigned)__builtin_amdgcn_readfirstlane(t[1]) << 32) | (unsigned)__builtin_amdgcn_readfirstlane(t[0]);
}
DI unsigned ring_next(unsigned x) { return x == 4 * 16384 ? 0u : x + 16384u; }
DI void duty_init(Duty& d, int w, int lane, rsrc_t ra, unsigned abase, unsigned arow, rsrc_t rw, unsigned wbase, unsigned wrow) {
  stg_x(d.x, ra, lane, abase, arow); stg_w(d.w, rw, lane, wbase, wrow);
  const int fx = w == 0 ? 0 : 1 + 2 * w;
  d.x.m0 += fx * 1024; d.x.m0alt += fx * 1024; d.x.so += fx * d.x.pstride;
  d.w.m0 += 2 * w * 1024; d.w.m0alt += 2 * w * 1024; d.w.so += 2 * w * d.w.pstride;
  d.w.vA = (w & 1) ? d.w.vB : d.w.vA;
}
template <int POL, int NX> DI void duty_stage(const Duty& d, int st, bool alt, bool dox = true, bool dow = true) {
  if (dow) stg_part<POL == 2 ? 2 : 0, 2>(d.w, st, 0, alt);
  if (dox) stg_part<POL, NX>(d.x, st, 0, alt);
}
template <int V> using IC = std::integral_constant<int, V>;

template <int HI, int NX> DI void phase1(int w, int lane, const GRd& gr, const Duty& d) {
  constexpr int NP = NX + 2;
  Ninth n; f32x4 acc[2][9];
  PIN(lane);
  duty_stage<0, NX>(d, 3, false);
  ninth_init(n, w, lane, L_Q9);
  acc_zero(acc);
  hintB<false, true, 0>(n, 0, gr, acc);
  unsigned cs = 0;
#pragma unroll 1
  for (int it = 0; it < 3; ++it) {
    BARRIER(); hintA<0, HI == 0, HI == 1, NX>(n, cs, d, 4 * it + 4); cs = ring_next(cs);
    WAITV(2 * NP); BARRIER(); hintB<HI == 0, true, 2>(n, cs, gr, acc); cs = ring_next(cs);
    BARRIER(); hintA<0, HI == 1, HI == 0, NX>(n, cs, d, 4 * it + 6); cs = ring_next(cs);
    WAITV(2 * NP); BARRIER(); hintB<HI == 1, true, 0>(n, cs, gr, acc); cs = ring_next(cs);
  }
  BARRIER(); hintA<0, HI == 0, HI == 1, -1>(n, cs, d, 0); cs = ring_next(cs);
  WAITV(0); BARRIER(); hintB<HI == 0, true, 2>(n, cs, gr, acc); cs = ring_next(cs);
  BARRIER(); hintA<0, HI == 1, HI == 0, -1>(n, cs, d, 0); cs = ring_next(cs);
  BARRIER(); hintB<HI == 1, false, 0>(n, cs, gr, acc);
  if constexpr (HI == 1) ninth_flush(n);
  PIN(lane);
  q_write(acc, w, lane, L_PA, L_Q9 + 4096);
  ninth_dump(n, w, lane, L_PB);
}
template <int HI, int NX> DI void phase2(int w, int lane, Ninth& n, const GRd& gr, f32x4 (&acc)[2][9], const Duty& d, u64* oflag, int nq, u64 tag) {
  constexpr int NP = NX + 2;
  acc_zero(acc);
  if constexpr (HI == 1) ninth_init(n, w, lane, L_Q9 + 4096);
  else hintB<true, false, 0>(n, 2 * 16384, gr, acc);
  WAITV(0);
  BARRIER();
  if (w == 0) st_flag(oflag + nq, tag);
#pragma unroll
  for (int st = 0; st < 4; ++st) duty_stage<2, NX>(d, st, false, false, true);
  hintA<0, HI == 1, HI == 0, -1>(n, 3 * 16384, d, 0);
  BARRIER();
  hintB<HI == 1, false, 0>(n, 4 * 16384, gr, acc);
  poll_flags(oflag, 4, nq, tag, lane);
  duty_stage<2, NX>(d, 0, false, true, false); duty_stage<2, NX>(d, 1, false, true, false);
  BARRIER();
  duty_stage<2, NX>(d, 2, false, true, false); duty_stage<2, NX>(d, 3, false, true, false);
  hintA<0, HI == 0, HI == 1, -1>(n, 0, d, 0);
  WAITV(2 * NX); BARRIER();
  hintB<HI == 0, true, 0>(n, 16384, gr, acc);
  unsigned cs = 2 * 16384;
  BARRIER(); hintA<2, HI == 1, HI == 0, NX>(n, cs, d, 4); cs = ring_next(cs);
  WAITV(2 * NP); BARRIER(); hintB<HI == 1, true, 2>(n, cs, gr, acc); cs = ring_next(cs);
#pragma unroll 1
  for (int it = 0; it < 2; ++it) {
    BARRIER(); hintA<2, HI == 0, HI == 1, NX>(n, cs, d, 4 * it + 6); cs = ring_next(cs);
    WAITV(2 * NP); BARRIER(); hintB<HI == 0, true, 0>(n, cs, gr, acc); cs = ring_next(cs);
    BARRIER(); hintA<2, HI == 1, HI == 0, NX>(n, cs, d, 4 * it + 8); cs = ring_next(cs);
    WAITV(2 * NP); BARRIER(); hintB<HI == 1, true, 2>(n, cs, gr, acc); cs = ring_next(cs);
  }
  STAMP(22);
  WAITV(0); BARRIER();
  duty_stage<2, NX>(d, 14, false); duty_stage<2, NX>(d, 15, false);
  PIN(lane);
  if constexpr (HI == 1) ninth_flush(n);
  ninth_dump(n, w, lane, L_QC);
  hintB<false, true, 0>(n, 0, gr, acc);
  BARRIER();
  WAITV(0); BARRIER();
  hintB<false, true, 2>(n, 0, gr, acc);
  BARRIER();
}
template <int NX> DI void tail_loader(int lane, const Duty& d, u64* oflag1, int nq, u64 tag) {
#pragma unroll
  for (int st = 0; st < 6; ++st) duty_stage<2, NX>(d, st, st < 4, false, true);
  BARRIER();
  duty_stage<2, NX>(d, 6, false, false, true); duty_stage<2, NX>(d, 7, false, false, true);
  poll_flags(oflag1, 4, nq, tag, lane);
#pragma unroll
  for (int st = 0; st < 8; ++st) duty_stage<2, NX>(d, st, st < 4, true, false);
#pragma unroll
  for (int dd = 0; dd < 16; ++dd) {
    const int hi = dd + 6 < 15 ? dd + 6 : 15;
    const int allowed = dd <= 7 ? (7 - dd) * NX + (dd >= 1 ? hi - 7 : 0) * (NX + 2) : (hi - dd) * (NX + 2);
    WAITV(allowed);
    BARRIER();
    if (dd >= 1 && dd + 7 < 16) duty_stage<2, NX>(d, dd + 7, ((dd + 7) & 4) == 0);
  }
}
template <int NX> DI void p0_loader(Duty& d, unsigned dh) {
  constexpr int NP = NX + 2;
#pragma unroll
  for (int st = 0; st < 4; ++st) duty_stage<0, NX>(d, st, false);
  WAITV(3 * NP); BARRIER();
#pragma unroll 1
  for (int be = 1; be < 16; ++be) {
    WAITV(2 * NP); BARRIER();
    if (be == 13) d.w.so += dh;
    duty_stage<0, NX>(d, (be + 3) & 15, false);
  }
  WAITV(NP);
}

__global__ __launch_bounds__(512, 2) void k_fused(const char* xA, const char* kA, const char* vA, const char* wqA, const char* woA,
                                                  const char* xB, const char* kB, const char* vB, const char* wqB, const char* woB,
                                                  char* ws, char* out) {
  extern __shared__ __attribute__((aligned(16))) char shm[];
  int lane = threadIdx.x & 63;
  const int wv = __builtin_amdgcn_readfirstlane(threadIdx.x >> 6);
  const int g = wv >> 2, w = wv & 3;
  const int bid = blockIdx.x, xcd = bid & 7, jj = bid >> 3;
  const int blk = xcd >> 2, hg = xcd & 3, b = jj >> 2, nq = jj & 3;
  const int hA = hg * 8 + 4 * (b & 1), h0 = hA + nq, h1 = h0 ^ 4;
  const rsrc_t rK = __builtin_amdgcn_make_buffer_rsrc((void*)(blk ? kB : kA), 0, NB * NH * SK * DH * 2, 0x00020000);
  const rsrc_t rV = __builtin_amdgcn_make_buffer_rsrc((void*)(blk ? vB : vA), 0, NB * NH * SK * DH * 2, 0x00020000);
  const rsrc_t rO = __builtin_amdgcn_make_buffer_rsrc((void*)(ws + WS_O), 0, 2 * NB * SQ * 8192, 0x00020000);
  const rsrc_t rWo = __builtin_amdgcn_make_buffer_rsrc((void*)(blk ? woB : woA), 0, NH * DH * DM * 2, 0x00020000);
  const unsigned base0 = (unsigned)(b * NH + h0) * (SK * DH * 2), base1 = (unsigned)(b * NH + h1) * (SK * DH * 2);
  unsigned vkv = ((lane >> 2) & 3) * 256 + (lane >> 4) * 64 + (lane & 3) * 16;
  const unsigned obase = (unsigned)(blk * NB + b) * (SQ * 8192);
  u64* const flg = (u64*)(ws + WS_F);
  u64* const oflag = flg + F_O / 8 + ((blk * NB + b) * 4 + hg) * 8;
  const int tile = (blk * NB + b) * 4 + nq;
  u64* const xflag = flg + F_X / 8 + tile * 16;
  GRd gr;
  grd_init(gr, w, lane, L_SX, L_SW);
  f32x4 acc[2][9];
  acc_zero(acc);
  f32x16 o[4]; float l = 0.f;
  Ninth n;
  STAMP(0);
  if (g == 0) {
    if (wv == 0) {
      const u64 tg = ld_flag(flg + F_GEN / 8) + 1;
      *(LDS3 u32x2*)L_TAG = u32x2{(unsigned)tg, (unsigned)(tg >> 32)};
      st_flag(flg + F_START / 8 + bid, tg);
    }
    kv_issue4<true>(0, w, vkv, rK, base0); kv_issue4<false>(16384, w, vkv, rV, base0); kv_issue4<true>(32768, w, vkv, rK, base0 + 16384);
    BARRIER();
    STAMP(1);
    gemm16(gr, gr, acc);
    STAMP(17);
    PIN(lane);
    q_write(acc, w, lane, L_QB0, L_Q9);
    BARRIER();
    STAMP(20);
    WAITV(8);
    __builtin_amdgcn_s_setprio(2);
    attention<false>(lane, w, L_QB0 + w * 8192 + lane * 16, o, l, rK, rV, base0, base1, vkv);
    __builtin_amdgcn_s_setprio(0);
    STAMP(21);
    PIN(lane);
    const u64 tag = get_tag();
    grd_init(gr, w, lane, L_SX, L_SW);
    Duty d;
    duty_init(d, w, lane, rO, obase + hA * 256, 8192, rWo, hA * 128 * 1024 + nq * 256, 1024);
    if (w < 2) {
      ninth_init(n, w, lane, L_Q9 + 4096);
      hintA<0, true, false, -1>(n, 1 * 16384, d, 0);
    }
    WAITV(4);
    epilogue2(w, lane, rO, obase + h0 * 256, L_PB);
    BARRIER();
    epilogue1(w, lane, o, l, rO, obase + h0 * 256, L_PA);
    if (w >= 2) phase2<1, 2>(w, lane, n, gr, acc, d, oflag, nq, tag);
    else if (w == 1) phase2<0, 2>(w, lane, n, gr, acc, d, oflag, nq, tag);
    else phase2<0, 3>(w, lane, n, gr, acc, d, oflag, nq, tag);
    STAMP(23);
    PIN(lane);
    GRd ga;
    grd_init(ga, w, lane, L_AX, L_AW);
    BARRIER();
    STAMP(24);
    gemm16(ga, gr, acc);
    STAMP(25);
    PIN(lane);
    if (w == hg) {
#pragma unroll
      for (int t = 0; t < 2; ++t)
#pragma unroll
        for (int m = 0; m < 9; ++m) *(LDS3 f32x4*)(L_FIN + (t * 9 + m) * 1024 + lane * 16) = acc[t][m];
    } else {
      const rsrc_t rX = __builtin_amdgcn_make_buffer_rsrc((void*)(ws + WS_X), 0, 64 * 16 * 18432, 0x00020000);
      const unsigned so = (unsigned)((tile * 4 + w) * 4 + hg) * 18432;
#pragma unroll
      for (int t = 0; t < 2; ++t)
#pragma unroll
        for (int m = 0; m < 9; ++m) __builtin_amdgcn_raw_buffer_store_b128(__builtin_bit_cast(u32x4, acc[t][m]), rX, so + (t * 9 + m) * 1024 + lane * 16, 0, 16);
      WAITV(0);
      st_flag(xflag + w * 4 + hg, tag);
    }
  } else {
    Duty d;
    {
      const rsrc_t rX = __builtin_amdgcn_make_buffer_rsrc((void*)(blk ? xB : xA), 0, NB * SQ * DM * 2, 0x00020000);
      const rsrc_t rWq = __builtin_amdgcn_make_buffer_rsrc((void*)(blk ? wqB : wqA), 0, DM * NH * DH * 2, 0x00020000);
      duty_init(d, w, lane, rX, b * (SQ * DM * 2), 1024, rWq, h0 * 256, 8192);
    }
    if (w == 0) p0_loader<3>(d, (unsigned)(h1 - h0) * 256u); else p0_loader<2>(d, (unsigned)(h1 - h0) * 256u);
    BARRIER();
    if (w >= 2) phase1<1, 2>(w, lane, gr, d);
    else if (w == 1) phase1<0, 2>(w, lane, gr, d);
    else phase1<0, 3>(w, lane, gr, d);
    STAMP(21);
    __builtin_amdgcn_s_setprio(2);
    attention<true>(lane, w, L_PA + w * 8192 + lane * 16, o, l, rK, rV, base1, base1, vkv);
    __builtin_amdgcn_s_setprio(0);
    STAMP(22);
    PIN(lane);
    const u64 tag = get_tag();
    duty_init(d, w, lane, rO, obase + (hA ^ 4) * 256, 8192, rWo, (hA ^ 4) * 128 * 1024 + nq * 256, 1024);
    epilogue1(w, lane, o, l, rO, obase + h1 * 256, L_QA);
    BARRIER();
    epilogue2(w, lane, rO, obase + h1 * 256, L_QC);
    WAITV(0);
    BARRIER();
    if (w == 0) { st_flag(oflag + 4 + nq, tag); tail_loader<3>(lane, d, oflag + 4, nq, tag); }
    else tail_loader<2>(lane, d, oflag + 4, nq, tag);
    if (w == 0) poll_flags(xflag + hg * 4, 4, hg, tag, lane);
  }
  BARRIER();
  STAMP(26);
  PIN(lane);
  {
    const rsrc_t rX = __builtin_amdgcn_make_buffer_rsrc((void*)(ws + WS_X), 0, 64 * 16 * 18432, 0x00020000);
    const unsigned so = (unsigned)((tile * 4 + hg) * 4) * 18432;
    char* ob = out + ((size_t)(b * SQ + (lane & 15)) * 1024 + blk * 512 + nq * 128 + 32 * hg + 4 * (lane >> 4)) * 2;
#pragma unroll
    for (int r = 0; r < 3; ++r) {
      const int ti = wv + 8 * r;
      if (ti < 18) {
        f32x4 v = *(LDS3 f32x4*)(L_FIN + ti * 1024 + lane * 16);
#pragma unroll
        for (int sh = 1; sh < 4; ++sh) {
          const int src = (hg + sh) & 3;
          v += __builtin_bit_cast(f32x4, __builtin_amdgcn_raw_buffer_load_b128(rX, ti * 1024 + lane * 16, so + src * 18432, 16));
        }
        const int t = ti >= 9 ? 1 : 0, m = ti - 9 * t;
        *(u32x2*)(ob + (size_t)m * 16 * 2048 + t * 32) = u32x2{cvtpk(v[0], v[1]), cvtpk(v[2], v[3])};
      }
    }
  }
  if (bid == 0 && wv == 0) {
    const u64 tag = get_tag();
#pragma unroll
    for (int i = 0; i < 4; ++i) poll_flags(flg + F_START / 8 + i * 64, 64, -1, tag, lane);
    st_flag(flg + F_GEN / 8, tag);
  }
  STAMP(38);
  STAMP(39);
}

extern "C" void kernel_launch(void* const* d_in, const int* in_sizes, int n_in, void* d_out, int out_size, void* d_ws, size_t ws_size, hipStream_t stream) {
  (void)hipFuncSetAttribute((const void*)k_fused, hipFuncAttributeMaxDynamicSharedMemorySize, LDS_BYTES);
  k_fused<<<256, 512, LDS_BYTES, stream>>>((const char*)d_in[0], (const char*)d_in[1], (const char*)d_in[2], (const char*)d_in[3], (const char*)d_in[4],
                                           (const char*)d_in[5], (const char*)d_in[6], (const char*)d_in[7], (const char*)d_in[8], (const char*)d_in[9],
                                           (char*)d_ws, (char*)d_out);
}
```

```cpp
#include <hip/hip_runtime.h>
#include <hip/hip_bf16.h>
#include <type_traits>

typedef short bf16x8 __attribute__((ext_vector_type(8)));
typedef short bf16x4 __attribute__((ext_vector_type(4)));
typedef float f32x16 __attribute__((ext_vector_type(16)));
typedef float f32x4 __attribute__((ext_vector_type(4)));
typedef float f32x2 __attribute__((ext_vector_type(2)));
typedef __bf16 bfx2 __attribute__((ext_vector_type(2)));
typedef unsigned u32x4 __attribute__((ext_vector_type(4)));
typedef unsigned u32x2 __attribute__((ext_vector_type(2)));
#define LDS3 __attribute__((address_space(3)))
#define DI __device__ __forceinline__
typedef __amdgpu_buffer_rsrc_t rsrc_t;

constexpr int NB = 8, SQ = 144, DM = 512, NH = 32, SK = 512, DH = 128;
constexpr float CSC = 0.08838834764831845f * 1.4426950408889634f;
constexpr float THR = 6.0f;

constexpr unsigned L_RING = 0;
constexpr unsigned L_SX = 81920;
constexpr unsigned L_SW = 118784;
constexpr unsigned L_Q9 = 151552;
constexpr unsigned L_ST = 159744;
constexpr unsigned L_TAG = 160512;
constexpr unsigned L_QB0 = 49152;
constexpr unsigned L_PA = 81920;
constexpr unsigned L_PB = 118784;
constexpr unsigned L_QA = 0;
constexpr unsigned L_QC = 36864;
constexpr unsigned L_FIN = 0;
constexpr unsigned LDS_BYTES = 163840;
constexpr size_t WS_O = 0;
constexpr size_t WS_X = (size_t)2 * 8 * 144 * 8192;
constexpr size_t WS_F = WS_X + (size_t)64 * 16 * 18432;
constexpr size_t F_GEN = 0, F_START = 256, F_O = 4096, F_X = 16384;

DI unsigned cvtpk(float lo, float hi) { f32x2 v = {lo, hi}; bfx2 b = __builtin_convertvector(v, bfx2); return __builtin_bit_cast(unsigned, b); }
DI bf16x8 ld128(unsigned a) { return *(LDS3 bf16x8*)a; }
DI bf16x4 ldtr(unsigned a) { return __builtin_amdgcn_ds_read_tr16_b64_v4i16((LDS3 bf16x4*)a); }
DI bf16x8 cat(bf16x4 a, bf16x4 b) { return __builtin_shufflevector(a, b, 0, 1, 2, 3, 4, 5, 6, 7); }
DI f32x16 mfma32(bf16x8 a, bf16x8 b, f32x16 c) { return __builtin_amdgcn_mfma_f32_32x32x16_bf16(a, b, c, 0, 0, 0); }
DI f32x4 mfma16(bf16x8 a, bf16x8 b, f32x4 c) { return __builtin_amdgcn_mfma_f32_16x16x32_bf16(a, b, c, 0, 0, 0); }
DI float fmax3(float a, float b, float c) { return __builtin_elementwise_maximum(__builtin_elementwise_maximum(a, b), c); }
DI float fmax2(float a, float b) { return __builtin_elementwise_maximum(a, b); }
DI float ex2(float x) { return __builtin_amdgcn_exp2f(x); }
DI float sw32max(float x) { auto r = __builtin_amdgcn_permlane32_swap(__float_as_uint(x), __float_as_uint(x), false, false); return fmax2(__uint_as_float(r[0]), __uint_as_float(r[1])); }
DI float sw16max(float x) { auto r = __builtin_amdgcn_permlane16_swap(__float_as_uint(x), __float_as_uint(x), false, false); return fmax2(__uint_as_float(r[0]), __uint_as_float(r[1])); }
DI float sw32add(float x) { auto r = __builtin_amdgcn_permlane32_swap(__float_as_uint(x), __float_as_uint(x), false, false); return __uint_as_float(r[0]) + __uint_as_float(r[1]); }
DI float sw16add(float x) { auto r = __builtin_amdgcn_permlane16_swap(__float_as_uint(x), __float_as_uint(x), false, false); return __uint_as_float(r[0]) + __uint_as_float(r[1]); }

#define TV0()
#define TV1()
#define STAMP(i)
#define TBAR(acc) BARRIER()
#define BARRIER() asm volatile("s_waitcnt lgkmcnt(0)\n\ts_barrier" ::: "memory")
#define WAITV(n) asm volatile("s_waitcnt vmcnt(%0)" ::"i"(n) : "memory")
#define SB() __builtin_amdgcn_sched_barrier(0)
#define PIN(x) asm volatile("" : "+v"(x))
#define UNLIKELY(x) __builtin_expect(!!(x), 0)

template <int OFF, int POL> DI void dma(unsigned m0v, unsigned voff, rsrc_t r, unsigned soff) {
  if constexpr (POL == 1)
    asm volatile("s_mov_b32 m0, %0\n\ts_nop 0\n\tbuffer_load_dwordx4 %1, %2, %3 offen offset:%4 nt lds" ::"s"(m0v), "v"(voff), "s"(r), "s"(soff), "i"(OFF) : "memory");
  else if constexpr (POL == 2)
    asm volatile("s_mov_b32 m0, %0\n\ts_nop 0\n\tbuffer_load_dwordx4 %1, %2, %3 offen offset:%4 sc1 lds" ::"s"(m0v), "v"(voff), "s"(r), "s"(soff), "i"(OFF) : "memory");
  else
    asm volatile("s_mov_b32 m0, %0\n\ts_nop 0\n\tbuffer_load_dwordx4 %1, %2, %3 offen offset:%4 lds" ::"s"(m0v), "v"(voff), "s"(r), "s"(soff), "i"(OFF) : "memory");
}
template <int POL> DI void dma16(unsigned m0v, unsigned voff, rsrc_t r, unsigned soff) {
  if constexpr (POL == 2)
    asm volatile("s_mov_b32 m0, %0\n\ts_mov_b64 exec, 0xffff\n\tbuffer_load_dwordx4 %1, %2, %3 offen sc1 lds\n\ts_mov_b64 exec, -1" ::"s"(m0v), "v"(voff), "s"(r), "s"(soff) : "memory");
  else
    asm volatile("s_mov_b32 m0, %0\n\ts_mov_b64 exec, 0xffff\n\tbuffer_load_dwordx4 %1, %2, %3 offen lds\n\ts_mov_b64 exec, -1" ::"s"(m0v), "v"(voff), "s"(r), "s"(soff) : "memory");
}
template <bool ISK> DI void kv_issue4(unsigned slot_base, int wq, unsigned vkv, rsrc_t r, unsigned tile_off) {
  const unsigned m0v = slot_base + wq * 4096, so = tile_off + wq * 4096;
  if constexpr (ISK) { dma<0, 1>(m0v, vkv, r, so); dma<1024, 1>(m0v, vkv ^ 16u, r, so); dma<2048, 1>(m0v, vkv ^ 32u, r, so); dma<3072, 1>(m0v, vkv ^ 48u, r, so); }
  else { dma<0, 1>(m0v, vkv, r, so); dma<1024, 1>(m0v, vkv, r, so); dma<2048, 1>(m0v, vkv, r, so); dma<3072, 1>(m0v, vkv, r, so); }
}

template <bool ISV> DI void kv_h(unsigned sb, int hw, unsigned vkv, rsrc_t r, unsigned to) {
  kv_issue4<!ISV>(sb, 2 * hw, vkv, r, to); kv_issue4<!ISV>(sb, 2 * hw + 1, vkv, r, to);
}

struct Stg { rsrc_t r; unsigned vA, vB, m0, m0alt, rstride, so, sstride, pstride, last; };
struct GRd { unsigned xa, wa0, wa1; };
constexpr unsigned L_AX = 0, L_AW = 36864;
DI void stg_x(Stg& s, rsrc_t r, int lane, unsigned abase, unsigned arow) {
  const unsigned row16 = lane >> 2, sl = lane & 3;
  s.r = r; s.vA = s.vB = row16 * arow + (sl ^ ((0u - (row16 >> 2)) & 3)) * 16;
  s.m0 = L_SX; s.m0alt = L_AX; s.rstride = 9216; s.so = abase; s.sstride = 64; s.pstride = 16 * arow; s.last = 8;
}
DI void stg_w(Stg& s, rsrc_t r, int lane, unsigned wbase, unsigned wrow) {
  s.r = r; s.vA = ((lane >> 2) & 3) * wrow + (lane >> 4) * 64 + (lane & 3) * 16; s.vB = s.vA ^ 32u;
  s.m0 = L_SW; s.m0alt = L_AW; s.rstride = 8192; s.so = wbase; s.sstride = 32 * wrow; s.pstride = 4 * wrow; s.last = 0;
}
template <int POL> DI void stg_piece(const Stg& s, unsigned m0s, unsigned sos, int i) {
  if (i < 8) dma<0, POL>(m0s + i * 1024, (i & 2) ? s.vB : s.vA, s.r, sos + i * s.pstride);
  else dma<0, POL>(m0s + s.last * 1024, s.vA, s.r, sos + s.last * s.pstride);
}
template <int POL, int NP> DI void stg_part(const Stg& s, int st, int first, bool alt) {
  const unsigned m0s = (alt ? s.m0alt : s.m0) + (st & 3) * s.rstride + first * 1024, sos = s.so + st * s.sstride + first * s.pstride;
#pragma unroll
  for (int i = 0; i < NP; ++i) dma<0, POL>(m0s + i * 1024, (i & 2) ? s.vB : s.vA, s.r, sos + i * s.pstride);
}
template <int POL> DI void stg_stage(const Stg& s, int st, bool alt = false) {
  const unsigned m0s = (alt ? s.m0alt : s.m0) + (st & 3) * s.rstride, sos = s.so + st * s.sstride;
#pragma unroll
  for (int i = 0; i < 9; ++i) stg_piece<POL>(s, m0s, sos, i);
}
DI void grd_init(GRd& g, int w, int lane, unsigned xring, unsigned wring) {
  const unsigned g4 = lane >> 4;
  g.xa = xring + 64 * (lane & 15) + 16 * (g4 ^ ((0u - ((lane >> 2) & 3)) & 3));
  g.wa0 = wring + 2048 * g4 + 256 * w + 64 * ((lane >> 2) & 3) + 32 * (g4 & 1) + 8 * (lane & 3);
  g.wa1 = g.wa0 ^ 32u;
  PIN(g.xa); PIN(g.wa0); PIN(g.wa1);
}
DI bf16x8 g_xf(const GRd& g, int u, int m) { return ld128(g.xa + 9216 * u + 1024 * m); }
DI bf16x8 g_wf(const GRd& g, int u, int t) { const unsigned a = (t ? g.wa1 : g.wa0) + 8192 * u; return cat(ldtr(a), ldtr(a + 1024)); }

DI void gemm_step(const GRd& g, int u, f32x4 (&acc)[2][9]) {
  const bf16x8 w0 = g_wf(g, u, 0), w1 = g_wf(g, u, 1);
#pragma unroll
  for (int m = 0; m < 9; ++m) {
    const bf16x8 x = g_xf(g, u, m);
    acc[0][m] = mfma16(w0, x, acc[0][m]);
    acc[1][m] = mfma16(w1, x, acc[1][m]);
  }
}
struct KvPre { rsrc_t rK, rV; unsigned base, vkv, wq; };
template <bool LAST, bool KV = false> DI void gemm_body(const GRd& g, const GRd& gn, f32x4 (&acc)[2][9], bf16x8 (&wf)[2], bf16x8 (&xf)[9], const KvPre* kv = nullptr) {
#pragma unroll
  for (int u = 0; u < 4; ++u) {
#pragma unroll
    for (int m = 0; m < 5; ++m) {
      acc[0][m] = mfma16(wf[0], xf[m], acc[0][m]);
      if (m < 4) xf[5 + m] = g_xf(g, u, 5 + m);
      acc[1][m] = mfma16(wf[1], xf[m], acc[1][m]);
      SB();
      if constexpr (KV) {
        if (u < 3 && m < 4) {
          const unsigned m0 = u * 16384 + kv->wq, so = kv->base + (u >> 1) * 16384 + kv->wq, v = u == 1 ? kv->vkv : kv->vkv ^ (16u * m);
          if (m == 0) dma<0, 1>(m0, v, u == 1 ? kv->rV : kv->rK, so); else if (m == 1) dma<1024, 1>(m0, v, u == 1 ? kv->rV : kv->rK, so);
          else if (m == 2) dma<2048, 1>(m0, v, u == 1 ? kv->rV : kv->rK, so); else dma<3072, 1>(m0, v, u == 1 ? kv->rV : kv->rK, so);
          SB();
        }
      }
    }
    const bool nb = !(LAST && u == 3);
    if (nb) BARRIER();
    SB();
    const GRd& q = u == 3 ? gn : g;
    bf16x8 wn[2];
#pragma unroll
    for (int m = 5; m < 9; ++m) {
      acc[0][m] = mfma16(wf[0], xf[m], acc[0][m]);
      if (nb) { if (m == 5) wn[0] = g_wf(q, (u + 1) & 3, 0); if (m == 6) wn[1] = g_wf(q, (u + 1) & 3, 1); if (m >= 7) xf[2 * (m - 7) + 1] = g_xf(q, (u + 1) & 3, 2 * (m - 7) + 1); }
      acc[1][m] = mfma16(wf[1], xf[m], acc[1][m]);
      if (nb) { if (m == 5) xf[0] = g_xf(q, (u + 1) & 3, 0); if (m >= 7) xf[2 * (m - 7) + 2] = g_xf(q, (u + 1) & 3, 2 * (m - 7) + 2); }
      SB();
    }
    if (nb) { wf[0] = wn[0]; wf[1] = wn[1]; }
  }
}
template <int KVIT> DI void gemm16(const GRd& ga, const GRd& gb, f32x4 (&acc)[2][9], const KvPre* kv = nullptr) {
  bf16x8 wf[2], xf[9];
  wf[0] = g_wf(ga, 0, 0); wf[1] = g_wf(ga, 0, 1);
#pragma unroll
  for (int m = 0; m < 5; ++m) xf[m] = g_xf(ga, 0, m);
  SB();
  GRd c = ga, nx = gb;
  if constexpr (KVIT < 0) {
#pragma unroll 1
    for (int it = 0; it < 3; ++it) { gemm_body<false>(c, nx, acc, wf, xf); const GRd t = c; c = nx; nx = t; }
  } else {
#pragma unroll 1
    for (int it = 0; it < KVIT; ++it) gemm_body<false>(c, c, acc, wf, xf);
    gemm_body<false, true>(c, c, acc, wf, xf, kv);
#pragma unroll 1
    for (int it = KVIT + 1; it < 3; ++it) gemm_body<false>(c, c, acc, wf, xf);
  }
  gemm_body<true>(c, nx, acc, wf, xf);
}
DI void acc_zero(f32x4 (&acc)[2][9]) {
#pragma unroll
  for (int t = 0; t < 2; ++t)
#pragma unroll
    for (int m = 0; m < 9; ++m) acc[t][m] = f32x4{};
}
DI void q_write(const f32x4 (&acc)[2][9], int w, int lane, unsigned qb, unsigned q9b) {
  const int q16 = lane & 15, g4 = lane >> 4;
#pragma unroll
  for (int t = 0; t < 2; ++t)
#pragma unroll
    for (int m = 0; m < 9; ++m) {
      const f32x4 a = acc[t][m] * CSC;
      u32x2 v = {cvtpk(a[0], a[1]), cvtpk(a[2], a[3])};
      if (m < 8) *(LDS3 u32x2*)(qb + (m >> 1) * 8192 + (2 * w + t) * 1024 + (16 * (m & 1) + q16 + 32 * (g4 >> 1)) * 16 + 8 * (g4 & 1)) = v;
      else *(LDS3 u32x2*)(q9b + w * 1024 + (q16 + 16 * (2 * t + (g4 >> 1))) * 16 + 8 * (g4 & 1)) = v;
    }
}

template <bool SECOND> DI void attention(int lane, int w, unsigned qaddr, f32x16 (&o)[4], float& l,
                                         rsrc_t rK, rsrc_t rV, unsigned ubase, unsigned ubase_next, unsigned vkv) {
  bf16x8 qf[8];
  if constexpr (!SECOND) {
#pragma unroll
    for (int i = 0; i < 8; ++i) qf[i] = ld128(qaddr + i * 1024);
  }
  const int h = lane >> 5;
  const unsigned rk = lane & 31;
  const unsigned ka0 = 1024 * (rk >> 2) + 64 * (rk & 3) + 16 * (h ^ ((rk >> 2) & 3));
  const unsigned va = 1024 * h + 64 * ((lane >> 2) & 3) + 32 * ((lane >> 4) & 1) + 8 * (lane & 3);
#pragma unroll
  for (int c = 0; c < 4; ++c) o[c] = f32x16{};
  l = 0.f;
  f32x16 negm = {};
  float thr = -3e38f;
  const unsigned vk1 = vkv ^ 16u, vk2 = vkv ^ 32u, vk3 = vkv ^ 48u;
  unsigned long long wA = 0, wB = 0, wV = 0;
  int slot = SECOND ? 1 : 0;
  bf16x8 kf[16];
  unsigned kA, kB;
#define KLD(i) ld128((((i) & 1) ? kB : kA) + 8192 * ((i) >> 3) + 256 * (((i) & 7) >> 1))
#define VLOAD(j) cat(ldtr(vA + 4096 * ((j) >> 2) + 256 * ((j) & 3)), ldtr(vA + 4096 * ((j) >> 2) + 2048 + 256 * ((j) & 3)))
#define SREG(p, e) ((p) < 8 ? s0[2 * (p) + (e)] : s1[2 * ((p) - 8) + (e)])
#define EX(p) do { if ((p) < 8) { s0[2 * (p)] = ex2(s0[2 * (p)]); s0[2 * (p) + 1] = ex2(s0[2 * (p) + 1]); PIN(s0[2 * (p)]); PIN(s0[2 * (p) + 1]); } \
                   else { s1[2 * ((p) - 8)] = ex2(s1[2 * ((p) - 8)]); s1[2 * ((p) - 8) + 1] = ex2(s1[2 * ((p) - 8) + 1]); PIN(s1[2 * ((p) - 8)]); PIN(s1[2 * ((p) - 8) + 1]); } } while (0)
#define FIN(p) do { pw[(p) >> 2][(p) & 3] = cvtpk(SREG(p, 0), SREG(p, 1)); PIN(pw[(p) >> 2][(p) & 3]); \
                    if ((p) < 8) { ps0 += SREG(p, 0); ps0 += SREG(p, 1); PIN(ps0); } else { ps1 += SREG(p, 0); ps1 += SREG(p, 1); PIN(ps1); } } while (0)
  BARRIER();
  if constexpr (SECOND) {
#pragma unroll
    for (int i = 0; i < 8; ++i) qf[i] = ld128(qaddr + i * 1024);
  }
  { kA = slot * 16384 + ka0; PIN(kA); kB = kA ^ 32u; PIN(kB); slot = slot == 4 ? 0 : slot + 1; }
#pragma unroll
  for (int i = 0; i < 4; ++i) kf[i] = KLD(i);
  SB();
  unsigned isl = SECOND ? 4 * 16384 : 3 * 16384;
  for (int t = 0; t < 8; ++t) {
    const unsigned vso = (t < 7 ? ubase + (t + 1) * 16384 : ubase_next) + w * 4096;
    const unsigned kso = (t < 6 ? ubase + (t + 2) * 16384 : ubase_next + (t - 6) * 16384) + w * 4096;
    const unsigned vm0 = isl + w * 4096; isl = isl == 4 * 16384 ? 0 : isl + 16384;
    const unsigned km0 = isl + w * 4096; isl = isl == 4 * 16384 ? 0 : isl + 16384;
    const bool dov = !SECOND || t < 7, dok = !SECOND || t < 6;
    f32x16 s0, s1;
    u32x4 pw[4];
    bf16x8 vf[16];
    float mx = 0.f, ps0 = 0.f, ps1 = 0.f, adj1 = 0.f, al1 = 1.f;
    bool f0 = false, f1 = false;
    unsigned vA = 0;
#pragma unroll
    for (int i = 0; i < 16; ++i) {
      if (i + 4 < 16) kf[i + 4] = KLD(i + 4);
      if (i == 0) s0 = mfma32(kf[0], qf[0], negm);
      else if (i < 8) s0 = mfma32(kf[i], qf[i], s0);
      else if (i == 8) s1 = mfma32(kf[8], qf[0], negm);
      else s1 = mfma32(kf[i], qf[i & 7], s1);
      if (i >= 1 && i <= 8) {
        SB();
        if (i <= 4) { if (dov) { if (i == 1) dma<0, 1>(vm0, vkv, rV, vso); else if (i == 2) dma<1024, 1>(vm0, vkv, rV, vso); else if (i == 3) dma<2048, 1>(vm0, vkv, rV, vso); else dma<3072, 1>(vm0, vkv, rV, vso); } }
        else if (dok) { if (i == 5) dma<0, 1>(km0, vkv, rK, kso); else if (i == 6) dma<1024, 1>(km0, vk1, rK, kso); else if (i == 7) dma<2048, 1>(km0, vk2, rK, kso); else dma<3072, 1>(km0, vk3, rK, kso); }
      }
      if (i == 9) { mx = fmax3(s0[0], s0[1], s0[2]); mx = fmax3(mx, s0[3], s0[4]); mx = fmax3(mx, s0[5], s0[6]); mx = fmax3(mx, s0[7], s0[8]); PIN(mx); }
      if (i == 10) {
        mx = fmax3(mx, s0[9], s0[10]); mx = fmax3(mx, s0[11], s0[12]); mx = fmax3(mx, s0[13], s0[14]); mx = fmax2(mx, s0[15]);
        if (UNLIKELY(__any(mx > thr))) {
          mx = sw32max(mx);
          const bool first = thr < 0.f;
          const float d = first ? mx : fmax2(mx, 0.f), al = first ? 1.f : ex2(-d);
          thr = THR;
#pragma unroll
          for (int r = 0; r < 16; ++r) { s0[r] -= d; negm[r] -= d; }
#pragma unroll
          for (int c = 0; c < 4; ++c)
#pragma unroll
            for (int r = 0; r < 16; ++r) o[c][r] *= al;
          l *= al; adj1 = d; f0 = true;
        }
      }
      if (i >= 11) EX(i - 11);
      if (i >= 12) FIN(i - 12);
      if (i == 13) {
        TV0();
        if constexpr (SECOND) { if (UNLIKELY(t >= 6)) { if (t == 6) WAITV(8); else WAITV(0); } else WAITV(12); } else WAITV(12);
        TV1();
        TBAR(wB);
        vA = slot * 16384 + va; PIN(vA); slot = slot == 4 ? 0 : slot + 1;
        vf[0] = VLOAD(0);
      }
      if (i == 14) { vf[1] = VLOAD(1); vf[2] = VLOAD(2); }
      if (i == 15) vf[3] = VLOAD(3);
      SB();
    }
#pragma unroll
    for (int j = 0; j < 16; ++j) {
      const int sp = j >> 2, c = j & 3;
      if (j + 4 < 16) vf[j + 4] = VLOAD(j + 4);
      o[c] = mfma32(vf[j], __builtin_bit_cast(bf16x8, pw[sp]), o[c]);
      if (j < 3) EX(5 + j);
      if (j < 4) FIN(4 + j);
      if (j == 1) {
        if (UNLIKELY(f0)) {
#pragma unroll
          for (int r = 0; r < 16; ++r) s1[r] -= adj1;
        }
        mx = fmax3(s1[0], s1[1], s1[2]); mx = fmax3(mx, s1[3], s1[4]); mx = fmax3(mx, s1[5], s1[6]); mx = fmax3(mx, s1[7], s1[8]); PIN(mx);
      }
      if (j == 2) {
        mx = fmax3(mx, s1[9], s1[10]); mx = fmax3(mx, s1[11], s1[12]); mx = fmax3(mx, s1[13], s1[14]); mx = fmax2(mx, s1[15]);
        if (UNLIKELY(__any(mx > THR))) {
          mx = sw32max(mx);
          const float d = fmax2(mx, 0.f);
          al1 = ex2(-d);
#pragma unroll
          for (int r = 0; r < 16; ++r) { s1[r] -= d; negm[r] -= d; }
          f1 = true;
        }
      }
      if (j >= 3 && j < 11) EX(8 + j - 3);
      if (j >= 4 && j < 12) FIN(8 + j - 4);
      if (j == 7) {
        l += ps0;
        if (UNLIKELY(f1)) {
#pragma unroll
          for (int cc = 0; cc < 4; ++cc)
#pragma unroll
            for (int r = 0; r < 16; ++r) o[cc][r] *= al1;
          l *= al1;
        }
      }
      if (j == 13) {
        TV0();
        if constexpr (SECOND) { if (UNLIKELY(t >= 6)) { if (t == 6) WAITV(4); } else WAITV(8); } else WAITV(8);
        TV1();
        TBAR(wA);
        kA = slot * 16384 + ka0; PIN(kA); kB = kA ^ 32u; PIN(kB); slot = slot == 4 ? 0 : slot + 1;
        kf[0] = KLD(0); kf[1] = KLD(1);
      }
      if (j == 14) { kf[2] = KLD(2); kf[3] = KLD(3); }
      SB();
    }
    l += ps1;
  }
#undef KLD
#undef VLOAD
#undef EX
#undef FIN
#undef SREG
}

struct Ninth { f32x4 o9[8]; f32x4 sa; float m9, l9; bf16x8 p9; bf16x4 vl[8], vh[8]; bf16x8 kf[4]; unsigned k9a, v9a, q9a; };
DI void ninth_init(Ninth& n, int w, int lane, unsigned q9base) {
  const int g4 = lane >> 4;
  const unsigned r9 = 16 * w + (lane & 15);
  n.k9a = 1024 * (r9 >> 2) + 64 * (r9 & 3) + 16 * (g4 ^ ((r9 >> 2) & 3));
  n.v9a = 1024 * (4 * w + g4) + 64 * ((lane >> 2) & 3) + 8 * (lane & 3);
  n.q9a = q9base + lane * 16;
#pragma unroll
  for (int c = 0; c < 8; ++c) { n.o9[c] = f32x4{}; n.vl[c] = bf16x4{}; n.vh[c] = bf16x4{}; }
  n.m9 = -1e30f; n.l9 = 0.f; n.p9 = bf16x8{}; n.sa = f32x4{};
}
DI void ninth_pv(Ninth& n, int c) { n.o9[c] = mfma16(cat(n.vl[c], n.vh[c]), n.p9, n.o9[c]); }
struct Duty { Stg x, w; unsigned vq, qm, qs; int rot; };
#define SPLITM 4
template <int POL, int NX> DI void duty_piece(const Duty& d, int s, int i) {
  if constexpr (NX >= 0) {
    const int ks = (s + d.rot) & 15;
    if (i < 2) dma<0, POL>(d.w.m0 + (s & 3) * 8192 + i * 1024, d.w.vA, d.w.r, d.w.so + ks * d.w.sstride + i * d.w.pstride);
    else if (i < 4) dma<0, POL>(d.x.m0 + (s & 3) * 9216 + (i - 2) * 1024, d.x.vA, d.x.r, d.x.so + ks * 64 + (i - 2) * d.x.pstride);
    else dma16<POL>(d.x.m0 + d.qm + (s & 3) * 9216, d.vq, d.x.r, d.x.so + d.qs + ks * 64);
  }
}
template <int POL, int NIN, bool GM, int U, int NX>
DI void hintA(Ninth& n, unsigned cs, const GRd& g, f32x4 (&acc)[2][9], const bf16x8 (&wb)[2], const Duty& d, int st) {
  bf16x8 xf[9];
  if constexpr (GM) {
#pragma unroll
    for (int m = SPLITM; m < 9; ++m) xf[m] = g_xf(g, U, m);
  }
  if constexpr (NIN) {
    unsigned na = cs + n.k9a;
    PIN(na);
#pragma unroll
    for (int s = 0; s < 4; ++s) n.kf[s] = ld128(na + 256 * s);
  }
  SB();
  if constexpr (NIN == 1) { ninth_pv(n, 0); ninth_pv(n, 1); }
  SB();
  duty_piece<POL, NX>(d, st, 0);
  SB();
  if constexpr (NIN == 1) { ninth_pv(n, 2); ninth_pv(n, 3); }
  SB();
#pragma unroll
  for (int i = 1; i < 5; ++i) {
    if constexpr (GM) { const int m = SPLITM + i - 1; if (m < 9) { acc[0][m] = mfma16(wb[0], xf[m], acc[0][m]); acc[1][m] = mfma16(wb[1], xf[m], acc[1][m]); } }
    SB();
    duty_piece<POL, NX>(d, st, i);
    SB();
  }
  if constexpr (GM) {
#pragma unroll
    for (int m = SPLITM + 4; m < 9; ++m) { acc[0][m] = mfma16(wb[0], xf[m], acc[0][m]); acc[1][m] = mfma16(wb[1], xf[m], acc[1][m]); }
  }
}
template <int POL, int NIN, bool GM, int U, int NB2, int NX>
DI void hintB(Ninth& n, unsigned cs, const GRd& g, f32x4 (&acc)[2][9], bf16x8 (&wb)[2], const Duty& d, int st) {
  constexpr int NM = 9 + NB2;
  bf16x8 wf[2], xf[18], q9f[4];
  f32x4 sb = {};
  unsigned na = cs + n.v9a;
  PIN(na);
  if constexpr (NIN) {
#pragma unroll
    for (int s = 0; s < 4; ++s) q9f[s] = ld128(n.q9a + 1024 * s);
  }
  if constexpr (GM) { wf[0] = g_wf(g, U, 0); xf[0] = g_xf(g, U, 0); wf[1] = g_wf(g, U, 1); xf[1] = g_xf(g, U, 1); xf[2] = g_xf(g, U, 2); xf[3] = g_xf(g, U, 3); }
  SB();
  if constexpr (NIN == 1) { ninth_pv(n, 4); ninth_pv(n, 5); ninth_pv(n, 6); ninth_pv(n, 7); }
  SB();
#pragma unroll
  for (int m = 0; m < (NM > 12 ? NM : 12); ++m) {
    if constexpr (NIN) { if (m < 4) sb = mfma16(n.kf[m], q9f[m], sb); }
    if constexpr (GM) {
      if (m < NM) {
        const int mm = m % 9;
        acc[0][mm] = mfma16(m < 9 ? wf[0] : wb[0], xf[m], acc[0][mm]);
        if (m + 4 < NM) xf[m + 4] = g_xf(g, U + (m + 4) / 9, (m + 4) % 9);
        acc[1][mm] = mfma16(m < 9 ? wf[1] : wb[1], xf[m], acc[1][mm]);
        if (m == 3) wb[0] = g_wf(g, U + 1, 0);
        if (m == 4) wb[1] = g_wf(g, U + 1, 1);
      }
    }
    if constexpr (NIN != 0) {
      if (m >= 4 && m < 12) { const int c = m - 4; const bf16x4 v = ldtr(na + 256 * (c >> 1) + 32 * (c & 1)); if (NIN == 1) n.vl[c] = v; else n.vh[c] = v; }
    }
    if constexpr (NIN == 1) { if (m == 6) { n.sa = sb; PIN(n.sa); } }
    if constexpr (NIN == 2) {
      if (m == 6) {
        float mx9 = fmax3(fmax3(n.sa[0], n.sa[1], n.sa[2]), n.sa[3], fmax3(fmax3(sb[0], sb[1], sb[2]), sb[3], sb[3]));
        if (UNLIKELY(__any(mx9 > n.m9 + THR))) {
          mx9 = sw32max(sw16max(mx9));
          const float mn = fmax2(n.m9, mx9), al = ex2(n.m9 - mn);
          n.m9 = mn; n.l9 *= al;
#pragma unroll
          for (int c = 0; c < 8; ++c)
#pragma unroll
            for (int r = 0; r < 4; ++r) n.o9[c][r] *= al;
        }
      }
      if (m == 7) {
#pragma unroll
        for (int r = 0; r < 4; ++r) { n.sa[r] = ex2(n.sa[r] - n.m9); PIN(n.sa[r]); }
      }
      if (m == 8) {
#pragma unroll
        for (int r = 0; r < 4; ++r) { sb[r] = ex2(sb[r] - n.m9); PIN(sb[r]); }
      }
      if (m == 9) {
        n.l9 += ((n.sa[0] + n.sa[1]) + (n.sa[2] + n.sa[3])) + ((sb[0] + sb[1]) + (sb[2] + sb[3]));
        u32x4 p = {cvtpk(n.sa[0], n.sa[1]), cvtpk(n.sa[2], n.sa[3]), cvtpk(sb[0], sb[1]), cvtpk(sb[2], sb[3])};
        n.p9 = __builtin_bit_cast(bf16x8, p);
        PIN(n.p9); PIN(n.l9);
      }
    }
    SB();
    if ((m & 1) == 1 && m < 10) duty_piece<POL, NX>(d, st, m >> 1);
    SB();
  }
}
DI void ninth_flush(Ninth& n) {
#pragma unroll
  for (int c = 0; c < 8; ++c) ninth_pv(n, c);
}
DI void ninth_dump(Ninth& n, int w, int lane, unsigned reg) {
  const int g4 = lane >> 4;
  const float l9s = sw32add(sw16add(n.l9));
  const unsigned p9 = reg + w * 8192 + (lane & 15) * 512;
#pragma unroll
  for (int c = 0; c < 8; ++c) *(LDS3 f32x4*)(p9 + 16 * ((4 * c + g4) ^ (lane & 15))) = n.o9[c];
  *(LDS3 f32x2*)(L_ST + (w * 16 + (lane & 15)) * 8) = f32x2{n.m9, l9s};
}

DI void epilogue1(int w, int lane, f32x16 (&o)[4], float l, rsrc_t rO, unsigned ounit, unsigned img) {
  const int h = lane >> 5, q = lane & 31, g4 = lane >> 4;
  const float rl = 1.0f / sw32add(l);
  const unsigned wa = img + 64 * (32 * w + q) + 8 * h, hx = (0u - (q >> 2)) & 3;
#pragma unroll
  for (int c = 0; c < 4; ++c)
#pragma unroll
    for (int i = 0; i < 4; ++i) {
      u32x2 v = {cvtpk(o[c][4 * i] * rl, o[c][4 * i + 1] * rl), cvtpk(o[c][4 * i + 2] * rl, o[c][4 * i + 3] * rl)};
      *(LDS3 u32x2*)(wa + 9216 * c + 16 * (i ^ hx)) = v;
    }
  asm volatile("s_waitcnt lgkmcnt(0)" ::: "memory");
#pragma unroll
  for (int j = 0; j < 8; ++j) {
    const unsigned row = 4 * j + g4;
    const u32x4 v = *(LDS3 u32x4*)(img + 9216 * ((lane >> 2) & 3) + 64 * (32 * w + row) + 16 * ((lane & 3) ^ ((0u - j) & 3)));
    __builtin_amdgcn_raw_buffer_store_b128(v, rO, ounit + (32 * w + row) * 8192 + (lane & 15) * 16, 0, 16);
  }
}
DI u32x4 merge9(int w, int lane, unsigned reg) {
  const int tid = w * 64 + lane, q = tid >> 4, dc = tid & 15;
  float mm[4], ll[4];
#pragma unroll
  for (int i = 0; i < 4; ++i) { mm[i] = *(LDS3 float*)(L_ST + (i * 16 + q) * 8); ll[i] = *(LDS3 float*)(L_ST + (i * 16 + q) * 8 + 4); }
  const float M = fmax2(fmax2(mm[0], mm[1]), fmax2(mm[2], mm[3]));
  float L = 0.f, sc[4];
#pragma unroll
  for (int i = 0; i < 4; ++i) { sc[i] = ex2(mm[i] - M); L += ll[i] * sc[i]; }
  const float rl = 1.0f / L;
  f32x4 a = {}, b = {};
#pragma unroll
  for (int i = 0; i < 4; ++i) {
    const unsigned p = reg + i * 8192 + q * 512;
    const f32x4 x = *(LDS3 f32x4*)(p + 16 * ((2 * dc) ^ q)), y = *(LDS3 f32x4*)(p + 16 * ((2 * dc + 1) ^ q));
    a += x * (sc[i] * rl); b += y * (sc[i] * rl);
  }
  return u32x4{cvtpk(a[0], a[1]), cvtpk(a[2], a[3]), cvtpk(b[0], b[1]), cvtpk(b[2], b[3])};
}
DI void store9(int w, int lane, u32x4 v, rsrc_t rO, unsigned ounit, unsigned img) {
  const int tid = w * 64 + lane, q = tid >> 4, dc = tid & 15;
  *(LDS3 u32x4*)(img + 9216 * (dc >> 2) + 64 * (128 + q) + 16 * ((dc & 3) ^ ((0u - w) & 3))) = v;
  __builtin_amdgcn_raw_buffer_store_b128(v, rO, ounit + (128 + q) * 8192 + dc * 16, 0, 16);
}

#define QROT (4 * ((nq + (b >> 1)) & 3))
#define KVIT_ 2
#define MPRIO 2
typedef unsigned long long u64;
DI u64 ld_flag(const u64* p) { return __hip_atomic_load(p, __ATOMIC_RELAXED, __HIP_MEMORY_SCOPE_AGENT); }
DI void st_flag(u64* p, u64 v) { __hip_atomic_store(p, v, __ATOMIC_RELAXED, __HIP_MEMORY_SCOPE_AGENT); }
DI void poll_flags(const u64* p, int n, int skip, u64 tag, int lane) {
  const bool act = lane < n && lane != skip;
  for (int it = 0; it < (1 << 20); ++it) {
    const u64 v = act ? ld_flag(p + lane) : tag;
    if (__all(v == tag)) break;
    __builtin_amdgcn_s_sleep(2);
  }
  asm volatile("" ::: "memory");
}
DI u64 get_tag() {
  const u32x2 t = *(LDS3 u32x2*)L_TAG;
  return ((u64)(unsigned)__builtin_amdgcn_readfirstlane(t[1]) << 32) | (unsigned)__builtin_amdgcn_readfirstlane(t[0]);
}
DI unsigned ring_next(unsigned x) { return x == 4 * 16384 ? 0u : x + 16384u; }
DI void duty_init(Duty& d, int w, int lane, rsrc_t ra, unsigned abase, unsigned arow, rsrc_t rw, unsigned wbase, unsigned wrow, int rot) {
  d.rot = rot;
  stg_x(d.x, ra, lane, abase, arow); stg_w(d.w, rw, lane, wbase, wrow);
  const int fx = 2 * w;
  d.vq = ((lane >> 2) & 3) * arow + ((lane & 3) ^ ((0u - w) & 3)) * 16;
  d.qm = 8192 + 256 * w - fx * 1024; d.qs = (128 + 4 * w) * arow - fx * d.x.pstride;
  d.x.m0 += fx * 1024; d.x.m0alt += fx * 1024; d.x.so += fx * d.x.pstride;
  d.w.m0 += 2 * w * 1024; d.w.m0alt += 2 * w * 1024; d.w.so += 2 * w * d.w.pstride;
  d.w.vA = (w & 1) ? d.w.vB : d.w.vA;
}
template <int POL> DI void duty_w(const Duty& d, int st, bool alt) {
  const unsigned m = (alt ? d.w.m0alt : d.w.m0) + (st & 3) * 8192, so = d.w.so + ((st + d.rot) & 15) * d.w.sstride;
  dma<0, POL>(m, d.w.vA, d.w.r, so); dma<0, POL>(m + 1024, d.w.vA, d.w.r, so + d.w.pstride);
}
template <int POL> DI void duty_x(const Duty& d, int st, bool alt) {
  const unsigned m = (alt ? d.x.m0alt : d.x.m0) + (st & 3) * 9216, so = d.x.so + ((st + d.rot) & 15) * 64;
  dma<0, POL>(m, d.x.vA, d.x.r, so); dma<0, POL>(m + 1024, d.x.vA, d.x.r, so + d.x.pstride);
  dma16<POL>(m + d.qm, d.vq, d.x.r, so + d.qs);
}
template <int POL, int NX> DI void duty_stage(const Duty& d, int st, bool alt) { duty_w<POL>(d, st, alt); duty_x<POL>(d, st, alt); }
template <int V> using IC = std::integral_constant<int, V>;

template <int NX> DI void phase1(int w, int lane, const GRd& gr, const Duty& d) {
  constexpr int NP = NX + 2;
  Ninth n; f32x4 acc[2][9]; bf16x8 wb[2];
  PIN(lane);
  duty_stage<0, NX>(d, 3, false);
  ninth_init(n, w, lane, L_Q9);
  acc_zero(acc);
  hintB<0, 0, true, 0, SPLITM, -1>(n, 0, gr, acc, wb, d, 0);
  unsigned cs = 0;
#pragma unroll 1
  for (int it = 0; it < 3; ++it) {
    BARRIER(); hintA<0, 1, true, 1, NX>(n, cs, gr, acc, wb, d, 4 * it + 4); cs = ring_next(cs);
    WAITV(NP); BARRIER(); hintB<0, 1, true, 2, SPLITM, NX>(n, cs, gr, acc, wb, d, 4 * it + 5); cs = ring_next(cs);
    BARRIER(); hintA<0, 2, true, 3, NX>(n, cs, gr, acc, wb, d, 4 * it + 6); cs = ring_next(cs);
    WAITV(NP); BARRIER(); hintB<0, 2, true, 0, SPLITM, NX>(n, cs, gr, acc, wb, d, 4 * it + 7); cs = ring_next(cs);
  }
  BARRIER(); hintA<0, 1, true, 1, -1>(n, cs, gr, acc, wb, d, 0); cs = ring_next(cs);
  WAITV(0); BARRIER(); hintB<0, 1, true, 2, SPLITM, -1>(n, cs, gr, acc, wb, d, 0); cs = ring_next(cs);
  BARRIER(); hintA<0, 2, true, 3, -1>(n, cs, gr, acc, wb, d, 0); cs = ring_next(cs);
  BARRIER(); hintB<0, 2, false, 0, 0, -1>(n, cs, gr, acc, wb, d, 0);
  ninth_flush(n);
  PIN(lane);
  q_write(acc, w, lane, L_PA, L_Q9 + 4096);
  ninth_dump(n, w, lane, L_PB);
}
template <int NX> DI void phase2(int w, int lane, Ninth& n, const GRd& gr, f32x4 (&acc)[2][9], bf16x8 (&wb)[2], const Duty& d) {
  constexpr int NP = NX + 2;
  unsigned cs = 0;
#pragma unroll 1
  for (int it = 0; it < 3; ++it) {
    BARRIER(); hintA<2, 1, true, 1, NX>(n, cs, gr, acc, wb, d, 4 * it + 4); cs = ring_next(cs);
    WAITV(NP); BARRIER(); hintB<2, 1, true, 2, SPLITM, NX>(n, cs, gr, acc, wb, d, 4 * it + 5); cs = ring_next(cs);
    BARRIER(); hintA<2, 2, true, 3, NX>(n, cs, gr, acc, wb, d, 4 * it + 6); cs = ring_next(cs);
    WAITV(NP); BARRIER(); hintB<2, 2, true, 0, SPLITM, NX>(n, cs, gr, acc, wb, d, 4 * it + 7); cs = ring_next(cs);
  }
  STAMP(22);
  BARRIER();
  ninth_flush(n);
  PIN(lane);
  ninth_dump(n, w, lane, L_QC);
  hintA<2, 0, true, 1, -1>(n, 0, gr, acc, wb, d, 0);
  WAITV(0); BARRIER();
  hintB<2, 0, true, 2, 9, -1>(n, 0, gr, acc, wb, d, 0);
}
template <int NX> DI void tail_loader(int lane, const Duty& d, u64* oflag1, int nq, u64 tag) {
#pragma unroll
  for (int st = 2; st < 8; ++st) duty_w<2>(d, st, st >= 4);
  BARRIER();
  poll_flags(oflag1, 4, nq, tag, lane);
  WAITV(0);
#pragma unroll
  for (int st = 4; st < 8; ++st) duty_x<2>(d, st, false);
  duty_w<2>(d, 8, false); duty_x<2>(d, 8, true);
#pragma unroll
  for (int dd = 2; dd < 16; ++dd) {
    const int hi = dd + 6 < 15 ? dd + 6 : 15;
    if (dd >= 4) WAITV(dd <= 7 ? (7 - dd) * 3 + (hi - 7) * 5 : (hi - dd) * 5);
    BARRIER();
    if (dd + 7 < 16) { duty_w<2>(d, dd + 7, ((dd + 7) & 4) != 0); duty_x<2>(d, dd + 7, ((dd + 7) & 4) == 0); }
  }
}
template <int NX> DI void p0_loader(Duty& d, unsigned dh) {
  constexpr int NP = NX + 2;
#pragma unroll
  for (int st = 0; st < 4; ++st) duty_stage<0, NX>(d, st, false);
  WAITV(3 * NP); BARRIER();
#pragma unroll 1
  for (int be = 1; be < 16; ++be) {
    WAITV(2 * NP); BARRIER();
    if (be == 13) d.w.so += dh;
    duty_stage<0, NX>(d, (be + 3) & 15, false);
  }
  WAITV(NP);
}

__global__ __launch_bounds__(512, 2) void k_fused(const char* xA, const char* kA, const char* vA, const char* wqA, const char* woA,
                                                  const char* xB, const char* kB, const char* vB, const char* wqB, const char* woB,
                                                  char* ws, char* out) {
  extern __shared__ __attribute__((aligned(16))) char shm[];
  int lane = threadIdx.x & 63;
  const int wv = __builtin_amdgcn_readfirstlane(threadIdx.x >> 6);
  const int g = wv >> 2, w = wv & 3;
  const int bid = blockIdx.x, xcd = bid & 7, jj = bid >> 3;
  const int blk = xcd >> 2, hg = xcd & 3, b = jj >> 2, nq = jj & 3;
  const int hA = hg * 8 + 4 * (b & 1), h0 = hA + nq, h1 = h0 ^ 4;
  const rsrc_t rK = __builtin_amdgcn_make_buffer_rsrc((void*)(blk ? kB : kA), 0, NB * NH * SK * DH * 2, 0x00020000);
  const rsrc_t rV = __builtin_amdgcn_make_buffer_rsrc((void*)(blk ? vB : vA), 0, NB * NH * SK * DH * 2, 0x00020000);
  const rsrc_t rO = __builtin_amdgcn_make_buffer_rsrc((void*)(ws + WS_O), 0, 2 * NB * SQ * 8192, 0x00020000);
  const rsrc_t rWo = __builtin_amdgcn_make_buffer_rsrc((void*)(blk ? woB : woA), 0, NH * DH * DM * 2, 0x00020000);
  const unsigned base0 = (unsigned)(b * NH + h0) * (SK * DH * 2), base1 = (unsigned)(b * NH + h1) * (SK * DH * 2);
  unsigned vkv = ((lane >> 2) & 3) * 256 + (lane >> 4) * 64 + (lane & 3) * 16;
  const unsigned obase = (unsigned)(blk * NB + b) * (SQ * 8192);
  u64* const flg = (u64*)(ws + WS_F);
  u64* const oflag = flg + F_O / 8 + ((blk * NB + b) * 4 + hg) * 8;
  const int tile = (blk * NB + b) * 4 + nq;
  u64* const xflag = flg + F_X / 8 + tile * 16;
  GRd gr;
  grd_init(gr, w, lane, L_SX, L_SW);
  f32x4 acc[2][9];
  acc_zero(acc);
  f32x16 o[4]; float l = 0.f;
  Ninth n;
  STAMP(0);
  if (g == 0) {
    const u64 gen = ld_flag(flg + F_GEN / 8);
    BARRIER();
    STAMP(1);
    KvPre kvp; kvp.rK = rK; kvp.rV = rV; kvp.base = base0; kvp.vkv = vkv; kvp.wq = w * 4096;
    gemm16<KVIT_>(gr, gr, acc, &kvp);
    STAMP(17);
    PIN(lane);
    q_write(acc, w, lane, L_QB0, L_Q9);
    if (wv == 0) {
      const u64 tg = gen + 1;
      *(LDS3 u32x2*)L_TAG = u32x2{(unsigned)tg, (unsigned)(tg >> 32)};
      st_flag(flg + F_START / 8 + bid, tg);
    }
    BARRIER();
    STAMP(20);
    WAITV(8);
    __builtin_amdgcn_s_setprio(MPRIO);
    attention<false>(lane, w, L_QB0 + w * 8192 + lane * 16, o, l, rK, rV, base0, base1, vkv);
    __builtin_amdgcn_s_setprio(0);
    STAMP(21);
    PIN(lane);
    const u64 tag = get_tag();
    grd_init(gr, w, lane, L_SX, L_SW);
    Duty d;
    duty_init(d, w, lane, rO, obase + hA * 256, 8192, rWo, hA * 128 * 1024 + nq * 256, 1024, 4 * nq);
    bf16x8 wb[2];
    ninth_init(n, w, lane, L_Q9 + 4096);
    hintA<0, 1, false, 0, -1>(n, 1 * 16384, gr, acc, wb, d, 0);
    const u32x4 r9 = merge9(w, lane, L_PB);
    WAITV(4);
    BARRIER();
    store9(w, lane, r9, rO, obase + h0 * 256, L_SX);
    epilogue1(w, lane, o, l, rO, obase + h0 * 256, L_SX);
#pragma unroll
    for (int st = 0; st < 4; ++st) duty_w<2>(d, st, false);
    hintB<0, 1, false, 0, 0, -1>(n, 2 * 16384, gr, acc, wb, d, 0);
    WAITV(8);
    BARRIER();
    if (w == 0) st_flag(oflag + nq, tag);
    acc_zero(acc);
    hintA<0, 2, false, 0, -1>(n, 3 * 16384, gr, acc, wb, d, 0);
    WAITV(0);
    BARRIER();
    hintB<0, 2, true, 0, SPLITM, -1>(n, 4 * 16384, gr, acc, wb, d, 0);
    poll_flags(oflag, 4, nq, tag, lane);
    phase2<3>(w, lane, n, gr, acc, wb, d);
    STAMP(23);
    PIN(lane);
    GRd ga, gb;
    grd_init(ga, w, lane, L_AX, L_SW);
    grd_init(gb, w, lane, L_SX, L_AW);
    BARRIER();
    STAMP(24);
    gemm16<-1>(ga, gb, acc);
    STAMP(25);
    PIN(lane);
    if (w == hg) {
#pragma unroll
      for (int t = 0; t < 2; ++t)
#pragma unroll
        for (int m = 0; m < 9; ++m) *(LDS3 f32x4*)(L_FIN + (t * 9 + m) * 1024 + lane * 16) = acc[t][m];
    } else {
      const rsrc_t rX = __builtin_amdgcn_make_buffer_rsrc((void*)(ws + WS_X), 0, 64 * 16 * 18432, 0x00020000);
      const unsigned so = (unsigned)((tile * 4 + w) * 4 + hg) * 18432;
#pragma unroll
      for (int t = 0; t < 2; ++t)
#pragma unroll
        for (int m = 0; m < 9; ++m) __builtin_amdgcn_raw_buffer_store_b128(__builtin_bit_cast(u32x4, acc[t][m]), rX, so + (t * 9 + m) * 1024 + lane * 16, 0, 16);
      WAITV(0);
      st_flag(xflag + w * 4 + hg, tag);
    }
  } else {
    Duty d;
    {
      const rsrc_t rX = __builtin_amdgcn_make_buffer_rsrc((void*)(blk ? xB : xA), 0, NB * SQ * DM * 2, 0x00020000);
      const rsrc_t rWq = __builtin_amdgcn_make_buffer_rsrc((void*)(blk ? wqB : wqA), 0, DM * NH * DH * 2, 0x00020000);
      duty_init(d, w, lane, rX, b * (SQ * DM * 2), 1024, rWq, h0 * 256, 8192, QROT);
    }
    p0_loader<3>(d, (unsigned)(h1 - h0) * 256u);
    BARRIER();
    phase1<3>(w, lane, gr, d);
    STAMP(21);
    __builtin_amdgcn_s_setprio(MPRIO);
    attention<true>(lane, w, L_PA + w * 8192 + lane * 16, o, l, rK, rV, base1, base1, vkv);
    __builtin_amdgcn_s_setprio(0);
    STAMP(22);
    PIN(lane);
    const u64 tag = get_tag();
    duty_init(d, w, lane, rO, obase + (hA ^ 4) * 256, 8192, rWo, (hA ^ 4) * 128 * 1024 + nq * 256, 1024, 4 * nq);
    duty_w<2>(d, 0, false);
    epilogue1(w, lane, o, l, rO, obase + h1 * 256, L_AX);
    BARRIER();
    duty_w<2>(d, 1, false);
    store9(w, lane, merge9(w, lane, L_QC), rO, obase + h1 * 256, L_AX);
    WAITV(0);
    BARRIER();
    if (w == 0) st_flag(oflag + 4 + nq, tag);
    tail_loader<3>(lane, d, oflag + 4, nq, tag);
    if (w == 0) poll_flags(xflag + hg * 4, 4, hg, tag, lane);
  }
  BARRIER();
  STAMP(26);
  PIN(lane);
  {
    const rsrc_t rX = __builtin_amdgcn_make_buffer_rsrc((void*)(ws + WS_X), 0, 64 * 16 * 18432, 0x00020000);
    const unsigned so = (unsigned)((tile * 4 + hg) * 4) * 18432;
    char* ob = out + ((size_t)(b * SQ + (lane & 15)) * 1024 + blk * 512 + nq * 128 + 32 * hg + 4 * (lane >> 4)) * 2;
#pragma unroll
    for (int r = 0; r < 3; ++r) {
      const int ti = wv + 8 * r;
      if (ti < 18) {
        f32x4 v = *(LDS3 f32x4*)(L_FIN + ti * 1024 + lane * 16);
#pragma unroll
        for (int sh = 1; sh < 4; ++sh) {
          const int src = (hg + sh) & 3;
          v += __builtin_bit_cast(f32x4, __builtin_amdgcn_raw_buffer_load_b128(rX, ti * 1024 + lane * 16, so + src * 18432, 16));
        }
        const int t = ti >= 9 ? 1 : 0, m = ti - 9 * t;
        *(u32x2*)(ob + (size_t)m * 16 * 2048 + t * 32) = u32x2{cvtpk(v[0], v[1]), cvtpk(v[2], v[3])};
      }
    }
  }
  if (bid == 0 && wv == 0) {
    const u64 tag = get_tag();
#pragma unroll
    for (int i = 0; i < 4; ++i) poll_flags(flg + F_START / 8 + i * 64, 64, -1, tag, lane);
    st_flag(flg + F_GEN / 8, tag);
  }
  STAMP(38);
  STAMP(39);
}

extern "C" void kernel_launch(void* const* d_in, const int* in_sizes, int n_in, void* d_out, int out_size, void* d_ws, size_t ws_size, hipStream_t stream) {
  (void)hipFuncSetAttribute((const void*)k_fused, hipFuncAttributeMaxDynamicSharedMemorySize, LDS_BYTES);
  k_fused<<<256, 512, LDS_BYTES, stream>>>((const char*)d_in[0], (const char*)d_in[1], (const char*)d_in[2], (const char*)d_in[3], (const char*)d_in[4],
                                           (const char*)d_in[5], (const char*)d_in[6], (const char*)d_in[7], (const char*)d_in[8], (const char*)d_in[9],
                                           (char*)d_ws, (char*)d_out);
}
```

```cpp
#include <hip/hip_runtime.h>
#include <hip/hip_bf16.h>
#include <type_traits>

typedef short bf16x8 __attribute__((ext_vector_type(8)));
typedef short bf16x4 __attribute__((ext_vector_type(4)));
typedef float f32x16 __attribute__((ext_vector_type(16)));
typedef float f32x4 __attribute__((ext_vector_type(4)));
typedef float f32x2 __attribute__((ext_vector_type(2)));
typedef __bf16 bfx2 __attribute__((ext_vector_type(2)));
typedef unsigned u32x4 __attribute__((ext_vector_type(4)));
typedef unsigned u32x2 __attribute__((ext_vector_type(2)));
#define LDS3 __attribute__((address_space(3)))
#define DI __device__ __forceinline__
typedef __amdgpu_buffer_rsrc_t rsrc_t;

constexpr int NB = 8, SQ = 144, DM = 512, NH = 32, SK = 512, DH = 128;
constexpr float CSC = 0.08838834764831845f * 1.4426950408889634f;
constexpr float THR = 6.0f;

constexpr unsigned L_RING = 0;
constexpr unsigned L_SX = 81920;
constexpr unsigned L_SW = 118784;
constexpr unsigned L_Q9 = 151552;
constexpr unsigned L_ST = 159744;
constexpr unsigned L_TAG = 160512;
constexpr unsigned L_QB0 = 49152;
constexpr unsigned L_PA = 81920;
constexpr unsigned L_PB = 118784;
constexpr unsigned L_QA = 0;
constexpr unsigned L_QC = 36864;
constexpr unsigned L_FIN = 0;
constexpr unsigned LDS_BYTES = 163840;
constexpr size_t WS_O = 0;
constexpr size_t WS_X = (size_t)2 * 8 * 144 * 8192;
constexpr size_t WS_F = WS_X + (size_t)64 * 16 * 18432;
constexpr size_t F_GEN = 0, F_START = 256, F_O = 4096, F_X = 16384;

DI unsigned cvtpk(float lo, float hi) { f32x2 v = {lo, hi}; bfx2 b = __builtin_convertvector(v, bfx2); return __builtin_bit_cast(unsigned, b); }
DI bf16x8 ld128(unsigned a) { return *(LDS3 bf16x8*)a; }
DI bf16x4 ldtr(unsigned a) { return __builtin_amdgcn_ds_read_tr16_b64_v4i16((LDS3 bf16x4*)a); }
DI bf16x8 cat(bf16x4 a, bf16x4 b) { return __builtin_shufflevector(a, b, 0, 1, 2, 3, 4, 5, 6, 7); }
DI f32x16 mfma32(bf16x8 a, bf16x8 b, f32x16 c) { return __builtin_amdgcn_mfma_f32_32x32x16_bf16(a, b, c, 0, 0, 0); }
DI f32x4 mfma16(bf16x8 a, bf16x8 b, f32x4 c) { return __builtin_amdgcn_mfma_f32_16x16x32_bf16(a, b, c, 0, 0, 0); }
DI float fmax3(float a, float b, float c) { return __builtin_elementwise_maximum(__builtin_elementwise_maximum(a, b), c); }
DI float fmax2(float a, float b) { return __builtin_elementwise_maximum(a, b); }
DI float ex2(float x) { return __builtin_amdgcn_exp2f(x); }
DI float sw32max(float x) { auto r = __builtin_amdgcn_permlane32_swap(__float_as_uint(x), __float_as_uint(x), false, false); return fmax2(__uint_as_float(r[0]), __uint_as_float(r[1])); }
DI float sw16max(float x) { auto r = __builtin_amdgcn_permlane16_swap(__float_as_uint(x), __float_as_uint(x), false, false); return fmax2(__uint_as_float(r[0]), __uint_as_float(r[1])); }
DI float sw32add(float x) { auto r = __builtin_amdgcn_permlane32_swap(__float_as_uint(x), __float_as_uint(x), false, false); return __uint_as_float(r[0]) + __uint_as_float(r[1]); }
DI float sw16add(float x) { auto r = __builtin_amdgcn_permlane16_swap(__float_as_uint(x), __float_as_uint(x), false, false); return __uint_as_float(r[0]) + __uint_as_float(r[1]); }

#define BARRIER() asm volatile("s_waitcnt lgkmcnt(0)\n\ts_barrier" ::: "memory")
#define WAITV(n) asm volatile("s_waitcnt vmcnt(%0)" ::"i"(n) : "memory")
#define SB() __builtin_amdgcn_sched_barrier(0)
#define PIN(x) asm volatile("" : "+v"(x))
#define UNLIKELY(x) __builtin_expect(!!(x), 0)

template <int OFF, int POL> DI void dma(unsigned m0v, unsigned voff, rsrc_t r, unsigned soff) {
  if constexpr (POL == 1)
    asm volatile("s_mov_b32 m0, %0\n\ts_nop 0\n\tbuffer_load_dwordx4 %1, %2, %3 offen offset:%4 nt lds" ::"s"(m0v), "v"(voff), "s"(r), "s"(soff), "i"(OFF) : "memory");
  else if constexpr (POL == 2)
    asm volatile("s_mov_b32 m0, %0\n\ts_nop 0\n\tbuffer_load_dwordx4 %1, %2, %3 offen offset:%4 sc1 lds" ::"s"(m0v), "v"(voff), "s"(r), "s"(soff), "i"(OFF) : "memory");
  else
    asm volatile("s_mov_b32 m0, %0\n\ts_nop 0\n\tbuffer_load_dwordx4 %1, %2, %3 offen offset:%4 lds" ::"s"(m0v), "v"(voff), "s"(r), "s"(soff), "i"(OFF) : "memory");
}
template <int POL> DI void dma16(unsigned m0v, unsigned voff, rsrc_t r, unsigned soff) {
  if constexpr (POL == 2)
    asm volatile("s_mov_b32 m0, %0\n\ts_mov_b64 exec, 0xffff\n\tbuffer_load_dwordx4 %1, %2, %3 offen sc1 lds\n\ts_mov_b64 exec, -1" ::"s"(m0v), "v"(voff), "s"(r), "s"(soff) : "memory");
  else
    asm volatile("s_mov_b32 m0, %0\n\ts_mov_b64 exec, 0xffff\n\tbuffer_load_dwordx4 %1, %2, %3 offen lds\n\ts_mov_b64 exec, -1" ::"s"(m0v), "v"(voff), "s"(r), "s"(soff) : "memory");
}
template <bool ISK> DI void kv_issue4(unsigned slot_base, int wq, unsigned vkv, rsrc_t r, unsigned tile_off) {
  const unsigned m0v = slot_base + wq * 4096, so = tile_off + wq * 4096;
  if constexpr (ISK) { dma<0, 1>(m0v, vkv, r, so); dma<1024, 1>(m0v, vkv ^ 16u, r, so); dma<2048, 1>(m0v, vkv ^ 32u, r, so); dma<3072, 1>(m0v, vkv ^ 48u, r, so); }
  else { dma<0, 1>(m0v, vkv, r, so); dma<1024, 1>(m0v, vkv, r, so); dma<2048, 1>(m0v, vkv, r, so); dma<3072, 1>(m0v, vkv, r, so); }
}

template <bool ISV> DI void kv_h(unsigned sb, int hw, unsigned vkv, rsrc_t r, unsigned to) {
  kv_issue4<!ISV>(sb, 2 * hw, vkv, r, to); kv_issue4<!ISV>(sb, 2 * hw + 1, vkv, r, to);
}

struct Stg { rsrc_t r; unsigned vA, vB, m0, m0alt, rstride, so, sstride, pstride, last; };
struct GRd { unsigned xa, wa0, wa1; };
constexpr unsigned L_AX = 0, L_AW = 36864;
DI void stg_x(Stg& s, rsrc_t r, int lane, unsigned abase, unsigned arow) {
  const unsigned row16 = lane >> 2, sl = lane & 3;
  s.r = r; s.vA = s.vB = row16 * arow + (sl ^ ((0u - (row16 >> 2)) & 3)) * 16;
  s.m0 = L_SX; s.m0alt = L_AX; s.rstride = 9216; s.so = abase; s.sstride = 64; s.pstride = 16 * arow; s.last = 8;
}
DI void stg_w(Stg& s, rsrc_t r, int lane, unsigned wbase, unsigned wrow) {
  s.r = r; s.vA = ((lane >> 2) & 3) * wrow + (lane >> 4) * 64 + (lane & 3) * 16; s.vB = s.vA ^ 32u;
  s.m0 = L_SW; s.m0alt = L_AW; s.rstride = 8192; s.so = wbase; s.sstride = 32 * wrow; s.pstride = 4 * wrow; s.last = 0;
}
template <int POL> DI void stg_piece(const Stg& s, unsigned m0s, unsigned sos, int i) {
  if (i < 8) dma<0, POL>(m0s + i * 1024, (i & 2) ? s.vB : s.vA, s.r, sos + i * s.pstride);
  else dma<0, POL>(m0s + s.last * 1024, s.vA, s.r, sos + s.last * s.pstride);
}
template <int POL, int NP> DI void stg_part(const Stg& s, int st, int first, bool alt) {
  const unsigned m0s = (alt ? s.m0alt : s.m0) + (st & 3) * s.rstride + first * 1024, sos = s.so + st * s.sstride + first * s.pstride;
#pragma unroll
  for (int i = 0; i < NP; ++i) dma<0, POL>(m0s + i * 1024, (i & 2) ? s.vB : s.vA, s.r, sos + i * s.pstride);
}
template <int POL> DI void stg_stage(const Stg& s, int st, bool alt = false) {
  const unsigned m0s = (alt ? s.m0alt : s.m0) + (st & 3) * s.rstride, sos = s.so + st * s.sstride;
#pragma unroll
  for (int i = 0; i < 9; ++i) stg_piece<POL>(s, m0s, sos, i);
}
DI void grd_init(GRd& g, int w, int lane, unsigned xring, unsigned wring) {
  const unsigned g4 = lane >> 4;
  g.xa = xring + 64 * (lane & 15) + 16 * (g4 ^ ((0u - ((lane >> 2) & 3)) & 3));
  g.wa0 = wring + 2048 * g4 + 256 * w + 64 * ((lane >> 2) & 3) + 32 * (g4 & 1) + 8 * (lane & 3);
  g.wa1 = g.wa0 ^ 32u;
  PIN(g.xa); PIN(g.wa0); PIN(g.wa1);
}
DI bf16x8 g_xf(const GRd& g, int u, int m) { return ld128(g.xa + 9216 * u + 1024 * m); }
DI bf16x8 g_wf(const GRd& g, int u, int t) { const unsigned a = (t ? g.wa1 : g.wa0) + 8192 * u; return cat(ldtr(a), ldtr(a + 1024)); }

DI void gemm_step(const GRd& g, int u, f32x4 (&acc)[2][9]) {
  const bf16x8 w0 = g_wf(g, u, 0), w1 = g_wf(g, u, 1);
#pragma unroll
  for (int m = 0; m < 9; ++m) {
    const bf16x8 x = g_xf(g, u, m);
    acc[0][m] = mfma16(w0, x, acc[0][m]);
    acc[1][m] = mfma16(w1, x, acc[1][m]);
  }
}
struct KvPre { rsrc_t rK, rV; unsigned base, vkv, wq; };
template <bool LAST, bool KV = false> DI void gemm_body(const GRd& g, const GRd& gn, f32x4 (&acc)[2][9], bf16x8 (&wf)[2], bf16x8 (&xf)[9], const KvPre* kv = nullptr) {
#pragma unroll
  for (int u = 0; u < 4; ++u) {
#pragma unroll
    for (int m = 0; m < 5; ++m) {
      acc[0][m] = mfma16(wf[0], xf[m], acc[0][m]);
      if (m < 4) xf[5 + m] = g_xf(g, u, 5 + m);
      acc[1][m] = mfma16(wf[1], xf[m], acc[1][m]);
      SB();
      if constexpr (KV) {
        if (u < 3 && m < 4) {
          const unsigned m0 = u * 16384 + kv->wq, so = kv->base + (u >> 1) * 16384 + kv->wq, v = u == 1 ? kv->vkv : kv->vkv ^ (16u * m);
          if (m == 0) dma<0, 1>(m0, v, u == 1 ? kv->rV : kv->rK, so); else if (m == 1) dma<1024, 1>(m0, v, u == 1 ? kv->rV : kv->rK, so);
          else if (m == 2) dma<2048, 1>(m0, v, u == 1 ? kv->rV : kv->rK, so); else dma<3072, 1>(m0, v, u == 1 ? kv->rV : kv->rK, so);
          SB();
        }
      }
    }
    const bool nb = !(LAST && u == 3);
    if (nb) BARRIER();
    SB();
    const GRd& q = u == 3 ? gn : g;
    bf16x8 wn[2];
#pragma unroll
    for (int m = 5; m < 9; ++m) {
      acc[0][m] = mfma16(wf[0], xf[m], acc[0][m]);
      if (nb) { if (m == 5) wn[0] = g_wf(q, (u + 1) & 3, 0); if (m == 6) wn[1] = g_wf(q, (u + 1) & 3, 1); if (m >= 7) xf[2 * (m - 7) + 1] = g_xf(q, (u + 1) & 3, 2 * (m - 7) + 1); }
      acc[1][m] = mfma16(wf[1], xf[m], acc[1][m]);
      if (nb) { if (m == 5) xf[0] = g_xf(q, (u + 1) & 3, 0); if (m >= 7) xf[2 * (m - 7) + 2] = g_xf(q, (u + 1) & 3, 2 * (m - 7) + 2); }
      SB();
    }
    if (nb) { wf[0] = wn[0]; wf[1] = wn[1]; }
  }
}
template <int KVIT> DI void gemm16(const GRd& ga, const GRd& gb, f32x4 (&acc)[2][9], const KvPre* kv = nullptr) {
  bf16x8 wf[2], xf[9];
  wf[0] = g_wf(ga, 0, 0); wf[1] = g_wf(ga, 0, 1);
#pragma unroll
  for (int m = 0; m < 5; ++m) xf[m] = g_xf(ga, 0, m);
  SB();
  GRd c = ga, nx = gb;
  if constexpr (KVIT < 0) {
#pragma unroll 1
    for (int it = 0; it < 3; ++it) { gemm_body<false>(c, nx, acc, wf, xf); const GRd t = c; c = nx; nx = t; }
  } else {
#pragma unroll 1
    for (int it = 0; it < KVIT; ++it) gemm_body<false>(c, c, acc, wf, xf);
    gemm_body<false, true>(c, c, acc, wf, xf, kv);
#pragma unroll 1
    for (int it = KVIT + 1; it < 3; ++it) gemm_body<false>(c, c, acc, wf, xf);
  }
  gemm_body<true>(c, nx, acc, wf, xf);
}
DI void acc_zero(f32x4 (&acc)[2][9]) {
#pragma unroll
  for (int t = 0; t < 2; ++t)
#pragma unroll
    for (int m = 0; m < 9; ++m) acc[t][m] = f32x4{};
}
DI void q_write(const f32x4 (&acc)[2][9], int w, int lane, unsigned qb, unsigned q9b) {
  const int q16 = lane & 15, g4 = lane >> 4;
#pragma unroll
  for (int t = 0; t < 2; ++t)
#pragma unroll
    for (int m = 0; m < 9; ++m) {
      const f32x4 a = acc[t][m] * CSC;
      u32x2 v = {cvtpk(a[0], a[1]), cvtpk(a[2], a[3])};
      if (m < 8) *(LDS3 u32x2*)(qb + (m >> 1) * 8192 + (2 * w + t) * 1024 + (16 * (m & 1) + q16 + 32 * (g4 >> 1)) * 16 + 8 * (g4 & 1)) = v;
      else *(LDS3 u32x2*)(q9b + w * 1024 + (q16 + 16 * (2 * t + (g4 >> 1))) * 16 + 8 * (g4 & 1)) = v;
    }
}

template <bool SECOND> DI void attention(int lane, int w, unsigned qaddr, f32x16 (&o)[4], float& l,
                                         rsrc_t rK, rsrc_t rV, unsigned ubase, unsigned ubase_next, unsigned vkv) {
  bf16x8 qf[8];
  if constexpr (!SECOND) {
#pragma unroll
    for (int i = 0; i < 8; ++i) qf[i] = ld128(qaddr + i * 1024);
  }
  const int h = lane >> 5;
  const unsigned rk = lane & 31;
  const unsigned ka0 = 1024 * (rk >> 2) + 64 * (rk & 3) + 16 * (h ^ ((rk >> 2) & 3));
  const unsigned va = 1024 * h + 64 * ((lane >> 2) & 3) + 32 * ((lane >> 4) & 1) + 8 * (lane & 3);
#pragma unroll
  for (int c = 0; c < 4; ++c) o[c] = f32x16{};
  l = 0.f;
  f32x16 negm = {};
  float thr = -3e38f;
  const unsigned vk1 = vkv ^ 16u, vk2 = vkv ^ 32u, vk3 = vkv ^ 48u;
  int slot = SECOND ? 1 : 0;
  bf16x8 kf[16];
  unsigned kA, kB;
#define KLD(i) ld128((((i) & 1) ? kB : kA) + 8192 * ((i) >> 3) + 256 * (((i) & 7) >> 1))
#define VLOAD(j) cat(ldtr(vA + 4096 * ((j) >> 2) + 256 * ((j) & 3)), ldtr(vA + 4096 * ((j) >> 2) + 2048 + 256 * ((j) & 3)))
#define SREG(p, e) ((p) < 8 ? s0[2 * (p) + (e)] : s1[2 * ((p) - 8) + (e)])
#define EX(p) do { if ((p) < 8) { s0[2 * (p)] = ex2(s0[2 * (p)]); s0[2 * (p) + 1] = ex2(s0[2 * (p) + 1]); PIN(s0[2 * (p)]); PIN(s0[2 * (p) + 1]); } \
                   else { s1[2 * ((p) - 8)] = ex2(s1[2 * ((p) - 8)]); s1[2 * ((p) - 8) + 1] = ex2(s1[2 * ((p) - 8) + 1]); PIN(s1[2 * ((p) - 8)]); PIN(s1[2 * ((p) - 8) + 1]); } } while (0)
#define FIN(p) do { pw[(p) >> 2][(p) & 3] = cvtpk(SREG(p, 0), SREG(p, 1)); PIN(pw[(p) >> 2][(p) & 3]); \
                    if ((p) < 8) { ps0 += SREG(p, 0); ps0 += SREG(p, 1); PIN(ps0); } else { ps1 += SREG(p, 0); ps1 += SREG(p, 1); PIN(ps1); } } while (0)
  BARRIER();
  if constexpr (SECOND) {
#pragma unroll
    for (int i = 0; i < 8; ++i) qf[i] = ld128(qaddr + i * 1024);
  }
  { kA = slot * 16384 + ka0; PIN(kA); kB = kA ^ 32u; PIN(kB); slot = slot == 4 ? 0 : slot + 1; }
#pragma unroll
  for (int i = 0; i < 4; ++i) kf[i] = KLD(i);
  SB();
  unsigned isl = SECOND ? 4 * 16384 : 3 * 16384;
  for (int t = 0; t < 8; ++t) {
    const unsigned vso = (t < 7 ? ubase + (t + 1) * 16384 : ubase_next) + w * 4096;
    const unsigned kso = (t < 6 ? ubase + (t + 2) * 16384 : ubase_next + (t - 6) * 16384) + w * 4096;
    const unsigned vm0 = isl + w * 4096; isl = isl == 4 * 16384 ? 0 : isl + 16384;
    const unsigned km0 = isl + w * 4096; isl = isl == 4 * 16384 ? 0 : isl + 16384;
    const bool dov = !SECOND || t < 7, dok = !SECOND || t < 6;
    f32x16 s0, s1;
    u32x4 pw[4];
    bf16x8 vf[16];
    float mx = 0.f, ps0 = 0.f, ps1 = 0.f, adj1 = 0.f, al1 = 1.f;
    bool f0 = false, f1 = false;
    unsigned vA = 0;
#pragma unroll
    for (int i = 0; i < 16; ++i) {
      if (i + 4 < 16) kf[i + 4] = KLD(i + 4);
      if (i == 0) s0 = mfma32(kf[0], qf[0], negm);
      else if (i < 8) s0 = mfma32(kf[i], qf[i], s0);
      else if (i == 8) s1 = mfma32(kf[8], qf[0], negm);
      else s1 = mfma32(kf[i], qf[i & 7], s1);
      if (i >= 1 && i <= 8) {
        SB();
        if (i <= 4) { if (dov) { if (i == 1) dma<0, 1>(vm0, vkv, rV, vso); else if (i == 2) dma<1024, 1>(vm0, vkv, rV, vso); else if (i == 3) dma<2048, 1>(vm0, vkv, rV, vso); else dma<3072, 1>(vm0, vkv, rV, vso); } }
        else if (dok) { if (i == 5) dma<0, 1>(km0, vkv, rK, kso); else if (i == 6) dma<1024, 1>(km0, vk1, rK, kso); else if (i == 7) dma<2048, 1>(km0, vk2, rK, kso); else dma<3072, 1>(km0, vk3, rK, kso); }
      }
      if (i == 9) { mx = fmax3(s0[0], s0[1], s0[2]); mx = fmax3(mx, s0[3], s0[4]); mx = fmax3(mx, s0[5], s0[6]); mx = fmax3(mx, s0[7], s0[8]); PIN(mx); }
      if (i == 10) {
        mx = fmax3(mx, s0[9], s0[10]); mx = fmax3(mx, s0[11], s0[12]); mx = fmax3(mx, s0[13], s0[14]); mx = fmax2(mx, s0[15]);
        if (UNLIKELY(__any(mx > thr))) {
          mx = sw32max(mx);
          const bool first = thr < 0.f;
          const float d = first ? mx : fmax2(mx, 0.f), al = first ? 1.f : ex2(-d);
          thr = THR;
#pragma unroll
          for (int r = 0; r < 16; ++r) { s0[r] -= d; negm[r] -= d; }
#pragma unroll
          for (int c = 0; c < 4; ++c)
#pragma unroll
            for (int r = 0; r < 16; ++r) o[c][r] *= al;
          l *= al; adj1 = d; f0 = true;
        }
      }
      if (i >= 11) EX(i - 11);
      if (i >= 12) FIN(i - 12);
      if (i == 13) {
        if constexpr (SECOND) { if (UNLIKELY(t >= 6)) { if (t == 6) WAITV(8); else WAITV(0); } else WAITV(12); } else WAITV(12);
        BARRIER();
        vA = slot * 16384 + va; PIN(vA); slot = slot == 4 ? 0 : slot + 1;
        vf[0] = VLOAD(0);
      }
      if (i == 14) { vf[1] = VLOAD(1); vf[2] = VLOAD(2); }
      if (i == 15) vf[3] = VLOAD(3);
      SB();
    }
#pragma unroll
    for (int j = 0; j < 16; ++j) {
      const int sp = j >> 2, c = j & 3;
      if (j + 4 < 16) vf[j + 4] = VLOAD(j + 4);
      o[c] = mfma32(vf[j], __builtin_bit_cast(bf16x8, pw[sp]), o[c]);
      if (j < 3) EX(5 + j);
      if (j < 4) FIN(4 + j);
      if (j == 1) {
        if (UNLIKELY(f0)) {
#pragma unroll
          for (int r = 0; r < 16; ++r) s1[r] -= adj1;
        }
        mx = fmax3(s1[0], s1[1], s1[2]); mx = fmax3(mx, s1[3], s1[4]); mx = fmax3(mx, s1[5], s1[6]); mx = fmax3(mx, s1[7], s1[8]); PIN(mx);
      }
      if (j == 2) {
        mx = fmax3(mx, s1[9], s1[10]); mx = fmax3(mx, s1[11], s1[12]); mx = fmax3(mx, s1[13], s1[14]); mx = fmax2(mx, s1[15]);
        if (UNLIKELY(__any(mx > THR))) {
          mx = sw32max(mx);
          const float d = fmax2(mx, 0.f);
          al1 = ex2(-d);
#pragma unroll
          for (int r = 0; r < 16; ++r) { s1[r] -= d; negm[r] -= d; }
          f1 = true;
        }
      }
      if (j >= 3 && j < 11) EX(8 + j - 3);
      if (j >= 4 && j < 12) FIN(8 + j - 4);
      if (j == 7) {
        l += ps0;
        if (UNLIKELY(f1)) {
#pragma unroll
          for (int cc = 0; cc < 4; ++cc)
#pragma unroll
            for (int r = 0; r < 16; ++r) o[cc][r] *= al1;
          l *= al1;
        }
      }
      if (j == 13) {
        if constexpr (SECOND) { if (UNLIKELY(t >= 6)) { if (t == 6) WAITV(4); } else WAITV(8); } else WAITV(8);
        BARRIER();
        kA = slot * 16384 + ka0; PIN(kA); kB = kA ^ 32u; PIN(kB); slot = slot == 4 ? 0 : slot + 1;
        kf[0] = KLD(0); kf[1] = KLD(1);
      }
      if (j == 14) { kf[2] = KLD(2); kf[3] = KLD(3); }
      SB();
    }
    l += ps1;
  }
#undef KLD
#undef VLOAD
#undef EX
#undef FIN
#undef SREG
}

struct Ninth { f32x4 o9[8]; f32x4 sa; float m9, l9; bf16x8 p9; bf16x4 vl[8], vh[8]; bf16x8 kf[4]; unsigned k9a, v9a, q9a; };
DI void ninth_init(Ninth& n, int w, int lane, unsigned q9base) {
  const int g4 = lane >> 4;
  const unsigned r9 = 16 * w + (lane & 15);
  n.k9a = 1024 * (r9 >> 2) + 64 * (r9 & 3) + 16 * (g4 ^ ((r9 >> 2) & 3));
  n.v9a = 1024 * (4 * w + g4) + 64 * ((lane >> 2) & 3) + 8 * (lane & 3);
  n.q9a = q9base + lane * 16;
#pragma unroll
  for (int c = 0; c < 8; ++c) { n.o9[c] = f32x4{}; n.vl[c] = bf16x4{}; n.vh[c] = bf16x4{}; }
  n.m9 = -1e30f; n.l9 = 0.f; n.p9 = bf16x8{}; n.sa = f32x4{};
}
DI void ninth_pv(Ninth& n, int c) { n.o9[c] = mfma16(cat(n.vl[c], n.vh[c]), n.p9, n.o9[c]); }
struct Duty { Stg x, w; unsigned vq, qm, qs; int rot; };
#define SPLITM 4
template <int POL, int NX> DI void duty_piece(const Duty& d, int s, int i) {
  if constexpr (NX >= 0) {
    const int ks = (s + d.rot) & 15;
    if (i < 2) dma<0, POL>(d.w.m0 + (s & 3) * 8192 + i * 1024, d.w.vA, d.w.r, d.w.so + ks * d.w.sstride + i * d.w.pstride);
    else if (i < 4) dma<0, POL>(d.x.m0 + (s & 3) * 9216 + (i - 2) * 1024, d.x.vA, d.x.r, d.x.so + ks * 64 + (i - 2) * d.x.pstride);
    else dma16<POL>(d.x.m0 + d.qm + (s & 3) * 9216, d.vq, d.x.r, d.x.so + d.qs + ks * 64);
  }
}
template <int POL, int NIN, bool GM, int U, int NX>
DI void hintA(Ninth& n, unsigned cs, const GRd& g, f32x4 (&acc)[2][9], const bf16x8 (&wb)[2], const Duty& d, int st) {
  bf16x8 xf[9];
  if constexpr (GM) {
#pragma unroll
    for (int m = SPLITM; m < 9; ++m) xf[m] = g_xf(g, U, m);
  }
  if constexpr (NIN) {
    unsigned na = cs + n.k9a;
    PIN(na);
#pragma unroll
    for (int s = 0; s < 4; ++s) n.kf[s] = ld128(na + 256 * s);
  }
  SB();
  if constexpr (NIN == 1) { ninth_pv(n, 0); ninth_pv(n, 1); }
  SB();
  duty_piece<POL, NX>(d, st, 0);
  SB();
  if constexpr (NIN == 1) { ninth_pv(n, 2); ninth_pv(n, 3); }
  SB();
#pragma unroll
  for (int i = 1; i < 5; ++i) {
    if constexpr (GM) { const int m = SPLITM + i - 1; if (m < 9) { acc[0][m] = mfma16(wb[0], xf[m], acc[0][m]); acc[1][m] = mfma16(wb[1], xf[m], acc[1][m]); } }
    SB();
    duty_piece<POL, NX>(d, st, i);
    SB();
  }
  if constexpr (GM) {
#pragma unroll
    for (int m = SPLITM + 4; m < 9; ++m) { acc[0][m] = mfma16(wb[0], xf[m], acc[0][m]); acc[1][m] = mfma16(wb[1], xf[m], acc[1][m]); }
  }
}
template <int POL, int NIN, bool GM, int U, int NB2, int NX>
DI void hintB(Ninth& n, unsigned cs, const GRd& g, f32x4 (&acc)[2][9], bf16x8 (&wb)[2], const Duty& d, int st) {
  constexpr int NM = 9 + NB2;
  bf16x8 wf[2], xf[18], q9f[4];
  f32x4 sb = {};
  unsigned na = cs + n.v9a;
  PIN(na);
  if constexpr (NIN) {
#pragma unroll
    for (int s = 0; s < 4; ++s) q9f[s] = ld128(n.q9a + 1024 * s);
  }
  if constexpr (GM) { wf[0] = g_wf(g, U, 0); xf[0] = g_xf(g, U, 0); wf[1] = g_wf(g, U, 1); xf[1] = g_xf(g, U, 1); xf[2] = g_xf(g, U, 2); xf[3] = g_xf(g, U, 3); }
  SB();
  if constexpr (NIN == 1) { ninth_pv(n, 4); ninth_pv(n, 5); ninth_pv(n, 6); ninth_pv(n, 7); }
  SB();
#pragma unroll
  for (int m = 0; m < (NM > 12 ? NM : 12); ++m) {
    if constexpr (NIN) { if (m < 4) sb = mfma16(n.kf[m], q9f[m], sb); }
    if constexpr (GM) {
      if (m < NM) {
        const int mm = m % 9;
        acc[0][mm] = mfma16(m < 9 ? wf[0] : wb[0], xf[m], acc[0][mm]);
        if (m + 4 < NM) xf[m + 4] = g_xf(g, U + (m + 4) / 9, (m + 4) % 9);
        acc[1][mm] = mfma16(m < 9 ? wf[1] : wb[1], xf[m], acc[1][mm]);
        if (m == 3) wb[0] = g_wf(g, U + 1, 0);
        if (m == 4) wb[1] = g_wf(g, U + 1, 1);
      }
    }
    if constexpr (NIN != 0) {
      if (m >= 4 && m < 12) { const int c = m - 4; const bf16x4 v = ldtr(na + 256 * (c >> 1) + 32 * (c & 1)); if (NIN == 1) n.vl[c] = v; else n.vh[c] = v; }
    }
    if constexpr (NIN == 1) { if (m == 6) { n.sa = sb; PIN(n.sa); } }
    if constexpr (NIN == 2) {
      if (m == 6) {
        float mx9 = fmax3(fmax3(n.sa[0], n.sa[1], n.sa[2]), n.sa[3], fmax3(fmax3(sb[0], sb[1], sb[2]), sb[3], sb[3]));
        if (UNLIKELY(__any(mx9 > n.m9 + THR))) {
          mx9 = sw32max(sw16max(mx9));
          const float mn = fmax2(n.m9, mx9), al = ex2(n.m9 - mn);
          n.m9 = mn; n.l9 *= al;
#pragma unroll
          for (int c = 0; c < 8; ++c)
#pragma unroll
            for (int r = 0; r < 4; ++r) n.o9[c][r] *= al;
        }
      }
      if (m == 7) {
#pragma unroll
        for (int r = 0; r < 4; ++r) { n.sa[r] = ex2(n.sa[r] - n.m9); PIN(n.sa[r]); }
      }
      if (m == 8) {
#pragma unroll
        for (int r = 0; r < 4; ++r) { sb[r] = ex2(sb[r] - n.m9); PIN(sb[r]); }
      }
      if (m == 9) {
        n.l9 += ((n.sa[0] + n.sa[1]) + (n.sa[2] + n.sa[3])) + ((sb[0] + sb[1]) + (sb[2] + sb[3]));
        u32x4 p = {cvtpk(n.sa[0], n.sa[1]), cvtpk(n.sa[2], n.sa[3]), cvtpk(sb[0], sb[1]), cvtpk(sb[2], sb[3])};
        n.p9 = __builtin_bit_cast(bf16x8, p);
        PIN(n.p9); PIN(n.l9);
      }
    }
    SB();
    if ((m & 1) == 1 && m < 10) duty_piece<POL, NX>(d, st, m >> 1);
    SB();
  }
}
DI void ninth_flush(Ninth& n) {
#pragma unroll
  for (int c = 0; c < 8; ++c) ninth_pv(n, c);
}
DI void ninth_dump(Ninth& n, int w, int lane, unsigned reg) {
  const int g4 = lane >> 4;
  const float l9s = sw32add(sw16add(n.l9));
  const unsigned p9 = reg + w * 8192 + (lane & 15) * 512;
#pragma unroll
  for (int c = 0; c < 8; ++c) *(LDS3 f32x4*)(p9 + 16 * ((4 * c + g4) ^ (lane & 15))) = n.o9[c];
  *(LDS3 f32x2*)(L_ST + (w * 16 + (lane & 15)) * 8) = f32x2{n.m9, l9s};
}

DI void epilogue1(int w, int lane, f32x16 (&o)[4], float l, rsrc_t rO, unsigned ounit, unsigned img) {
  const int h = lane >> 5, q = lane & 31, g4 = lane >> 4;
  const float rl = 1.0f / sw32add(l);
  const unsigned wa = img + 64 * (32 * w + q) + 8 * h, hx = (0u - (q >> 2)) & 3;
#pragma unroll
  for (int c = 0; c < 4; ++c)
#pragma unroll
    for (int i = 0; i < 4; ++i) {
      u32x2 v = {cvtpk(o[c][4 * i] * rl, o[c][4 * i + 1] * rl), cvtpk(o[c][4 * i + 2] * rl, o[c][4 * i + 3] * rl)};
      *(LDS3 u32x2*)(wa + 9216 * c + 16 * (i ^ hx)) = v;
    }
  asm volatile("s_waitcnt lgkmcnt(0)" ::: "memory");
#pragma unroll
  for (int j = 0; j < 8; ++j) {
    const unsigned row = 4 * j + g4;
    const u32x4 v = *(LDS3 u32x4*)(img + 9216 * ((lane >> 2) & 3) + 64 * (32 * w + row) + 16 * ((lane & 3) ^ ((0u - j) & 3)));
    __builtin_amdgcn_raw_buffer_store_b128(v, rO, ounit + (32 * w + row) * 8192 + (lane & 15) * 16, 0, 16);
  }
}
DI u32x4 merge9(int w, int lane, unsigned reg) {
  const int tid = w * 64 + lane, q = tid >> 4, dc = tid & 15;
  float mm[4], ll[4];
#pragma unroll
  for (int i = 0; i < 4; ++i) { mm[i] = *(LDS3 float*)(L_ST + (i * 16 + q) * 8); ll[i] = *(LDS3 float*)(L_ST + (i * 16 + q) * 8 + 4); }
  const float M = fmax2(fmax2(mm[0], mm[1]), fmax2(mm[2], mm[3]));
  float L = 0.f, sc[4];
#pragma unroll
  for (int i = 0; i < 4; ++i) { sc[i] = ex2(mm[i] - M); L += ll[i] * sc[i]; }
  const float rl = 1.0f / L;
  f32x4 a = {}, b = {};
#pragma unroll
  for (int i = 0; i < 4; ++i) {
    const unsigned p = reg + i * 8192 + q * 512;
    const f32x4 x = *(LDS3 f32x4*)(p + 16 * ((2 * dc) ^ q)), y = *(LDS3 f32x4*)(p + 16 * ((2 * dc + 1) ^ q));
    a += x * (sc[i] * rl); b += y * (sc[i] * rl);
  }
  return u32x4{cvtpk(a[0], a[1]), cvtpk(a[2], a[3]), cvtpk(b[0], b[1]), cvtpk(b[2], b[3])};
}
DI void store9(int w, int lane, u32x4 v, rsrc_t rO, unsigned ounit, unsigned img) {
  const int tid = w * 64 + lane, q = tid >> 4, dc = tid & 15;
  *(LDS3 u32x4*)(img + 9216 * (dc >> 2) + 64 * (128 + q) + 16 * ((dc & 3) ^ ((0u - w) & 3))) = v;
  __builtin_amdgcn_raw_buffer_store_b128(v, rO, ounit + (128 + q) * 8192 + dc * 16, 0, 16);
}

#define QROT (4 * ((nq + (b >> 1)) & 3))
#define KVIT_ 2
#define MPRIO 2
typedef unsigned long long u64;
DI u64 ld_flag(const u64* p) { return __hip_atomic_load(p, __ATOMIC_RELAXED, __HIP_MEMORY_SCOPE_AGENT); }
DI void st_flag(u64* p, u64 v) { __hip_atomic_store(p, v, __ATOMIC_RELAXED, __HIP_MEMORY_SCOPE_AGENT); }
DI void poll_flags(const u64* p, int n, int skip, u64 tag, int lane) {
  const int idx = lane & (n - 1);
  for (int it = 0; it < (1 << 16); ++it) {
    const u64 v = ld_flag(p + idx);
    if (__all(v == tag || idx == skip)) break;
    __builtin_amdgcn_s_sleep(2);
  }
  asm volatile("" ::: "memory");
}
DI u64 get_tag() {
  const u32x2 t = *(LDS3 u32x2*)L_TAG;
  return ((u64)(unsigned)__builtin_amdgcn_readfirstlane(t[1]) << 32) | (unsigned)__builtin_amdgcn_readfirstlane(t[0]);
}
DI unsigned ring_next(unsigned x) { return x == 4 * 16384 ? 0u : x + 16384u; }
DI void duty_init(Duty& d, int w, int lane, rsrc_t ra, unsigned abase, unsigned arow, rsrc_t rw, unsigned wbase, unsigned wrow, int rot) {
  d.rot = rot;
  stg_x(d.x, ra, lane, abase, arow); stg_w(d.w, rw, lane, wbase, wrow);
  const int fx = 2 * w;
  d.vq = ((lane >> 2) & 3) * arow + ((lane & 3) ^ ((0u - w) & 3)) * 16;
  d.qm = 8192 + 256 * w - fx * 1024; d.qs = (128 + 4 * w) * arow - fx * d.x.pstride;
  d.x.m0 += fx * 1024; d.x.m0alt += fx * 1024; d.x.so += fx * d.x.pstride;
  d.w.m0 += 2 * w * 1024; d.w.m0alt += 2 * w * 1024; d.w.so += 2 * w * d.w.pstride;
  d.w.vA = (w & 1) ? d.w.vB : d.w.vA;
}
template <int POL> DI void duty_w(const Duty& d, int st, bool alt) {
  const unsigned m = (alt ? d.w.m0alt : d.w.m0) + (st & 3) * 8192, so = d.w.so + ((st + d.rot) & 15) * d.w.sstride;
  dma<0, POL>(m, d.w.vA, d.w.r, so); dma<0, POL>(m + 1024, d.w.vA, d.w.r, so + d.w.pstride);
}
template <int POL> DI void duty_x(const Duty& d, int st, bool alt) {
  const unsigned m = (alt ? d.x.m0alt : d.x.m0) + (st & 3) * 9216, so = d.x.so + ((st + d.rot) & 15) * 64;
  dma<0, POL>(m, d.x.vA, d.x.r, so); dma<0, POL>(m + 1024, d.x.vA, d.x.r, so + d.x.pstride);
  dma16<POL>(m + d.qm, d.vq, d.x.r, so + d.qs);
}
template <int POL, int NX> DI void duty_stage(const Duty& d, int st, bool alt) { duty_w<POL>(d, st, alt); duty_x<POL>(d, st, alt); }
template <int V> using IC = std::integral_constant<int, V>;

template <int NX> DI void phase1(int w, int lane, const GRd& gr, const Duty& d) {
  constexpr int NP = NX + 2;
  Ninth n; f32x4 acc[2][9]; bf16x8 wb[2];
  PIN(lane);
  duty_stage<0, NX>(d, 3, false);
  ninth_init(n, w, lane, L_Q9);
  acc_zero(acc);
  hintB<0, 0, true, 0, SPLITM, -1>(n, 0, gr, acc, wb, d, 0);
  unsigned cs = 0;
#pragma unroll 1
  for (int it = 0; it < 3; ++it) {
    BARRIER(); hintA<0, 1, true, 1, NX>(n, cs, gr, acc, wb, d, 4 * it + 4); cs = ring_next(cs);
    WAITV(NP); BARRIER(); hintB<0, 1, true, 2, SPLITM, NX>(n, cs, gr, acc, wb, d, 4 * it + 5); cs = ring_next(cs);
    BARRIER(); hintA<0, 2, true, 3, NX>(n, cs, gr, acc, wb, d, 4 * it + 6); cs = ring_next(cs);
    WAITV(NP); BARRIER(); hintB<0, 2, true, 0, SPLITM, NX>(n, cs, gr, acc, wb, d, 4 * it + 7); cs = ring_next(cs);
  }
  BARRIER(); hintA<0, 1, true, 1, -1>(n, cs, gr, acc, wb, d, 0); cs = ring_next(cs);
  WAITV(0); BARRIER(); hintB<0, 1, true, 2, SPLITM, -1>(n, cs, gr, acc, wb, d, 0); cs = ring_next(cs);
  BARRIER(); hintA<0, 2, true, 3, -1>(n, cs, gr, acc, wb, d, 0); cs = ring_next(cs);
  BARRIER(); hintB<0, 2, false, 0, 0, -1>(n, cs, gr, acc, wb, d, 0);
  ninth_flush(n);
  PIN(lane);
  q_write(acc, w, lane, L_PA, L_Q9 + 4096);
  ninth_dump(n, w, lane, L_PB);
}
template <int NX> DI void phase2(int w, int lane, Ninth& n, const GRd& gr, f32x4 (&acc)[2][9], bf16x8 (&wb)[2], const Duty& d) {
  constexpr int NP = NX + 2;
  unsigned cs = 0;
#pragma unroll 1
  for (int it = 0; it < 3; ++it) {
    BARRIER(); hintA<2, 1, true, 1, NX>(n, cs, gr, acc, wb, d, 4 * it + 4); cs = ring_next(cs);
    WAITV(NP); BARRIER(); hintB<2, 1, true, 2, SPLITM, NX>(n, cs, gr, acc, wb, d, 4 * it + 5); cs = ring_next(cs);
    BARRIER(); hintA<2, 2, true, 3, NX>(n, cs, gr, acc, wb, d, 4 * it + 6); cs = ring_next(cs);
    WAITV(NP); BARRIER(); hintB<2, 2, true, 0, SPLITM, NX>(n, cs, gr, acc, wb, d, 4 * it + 7); cs = ring_next(cs);
  }
  BARRIER();
  ninth_flush(n);
  PIN(lane);
  ninth_dump(n, w, lane, L_QC);
  hintA<2, 0, true, 1, -1>(n, 0, gr, acc, wb, d, 0);
  WAITV(0); BARRIER();
  hintB<2, 0, true, 2, 9, -1>(n, 0, gr, acc, wb, d, 0);
}
template <int NX> DI void tail_loader(int lane, const Duty& d, u64* oflag1, int nq, u64 tag) {
#pragma unroll
  for (int st = 2; st < 8; ++st) duty_w<2>(d, st, st >= 4);
  BARRIER();
  poll_flags(oflag1, 4, nq, tag, lane);
  WAITV(0);
#pragma unroll
  for (int st = 4; st < 8; ++st) duty_x<2>(d, st, false);
  duty_w<2>(d, 8, false); duty_x<2>(d, 8, true);
#pragma unroll
  for (int dd = 2; dd < 16; ++dd) {
    const int hi = dd + 6 < 15 ? dd + 6 : 15;
    if (dd >= 4) WAITV(dd <= 7 ? (7 - dd) * 3 + (hi - 7) * 5 : (hi - dd) * 5);
    BARRIER();
    if (dd + 7 < 16) { duty_w<2>(d, dd + 7, ((dd + 7) & 4) != 0); duty_x<2>(d, dd + 7, ((dd + 7) & 4) == 0); }
  }
}
template <int NX> DI void p0_loader(Duty& d, unsigned dh) {
  constexpr int NP = NX + 2;
#pragma unroll
  for (int st = 0; st < 4; ++st) duty_stage<0, NX>(d, st, false);
  WAITV(3 * NP); BARRIER();
#pragma unroll 1
  for (int be = 1; be < 16; ++be) {
    WAITV(2 * NP); BARRIER();
    if (be == 13) d.w.so += dh;
    duty_stage<0, NX>(d, (be + 3) & 15, false);
  }
  WAITV(NP);
}

__global__ __launch_bounds__(512, 2) void k_fused(const char* xA, const char* kA, const char* vA, const char* wqA, const char* woA,
                                                  const char* xB, const char* kB, const char* vB, const char* wqB, const char* woB,
                                                  char* ws, char* out) {
  extern __shared__ __attribute__((aligned(16))) char shm[];
  unsigned long long pc; unsigned len;
  asm volatile("s_getpc_b64 %0\nkf_code_begin:\n\ts_mov_b32 %1, kf_code_end-kf_code_begin" : "=s"(pc), "=s"(len));
  int lane = threadIdx.x & 63;
  const int wv = __builtin_amdgcn_readfirstlane(threadIdx.x >> 6);
  const int g = wv >> 2, w = wv & 3;
  const int bid = blockIdx.x, xcd = bid & 7, jj = bid >> 3;
  const int blk = xcd >> 2, hg = xcd & 3, b = jj >> 2, nq = jj & 3;
  const int hA = hg * 8 + 4 * (b & 1), h0 = hA + nq, h1 = h0 ^ 4;
  const rsrc_t rK = __builtin_amdgcn_make_buffer_rsrc((void*)(blk ? kB : kA), 0, NB * NH * SK * DH * 2, 0x00020000);
  const rsrc_t rV = __builtin_amdgcn_make_buffer_rsrc((void*)(blk ? vB : vA), 0, NB * NH * SK * DH * 2, 0x00020000);
  const rsrc_t rO = __builtin_amdgcn_make_buffer_rsrc((void*)(ws + WS_O), 0, 2 * NB * SQ * 8192, 0x00020000);
  const rsrc_t rWo = __builtin_amdgcn_make_buffer_rsrc((void*)(blk ? woB : woA), 0, NH * DH * DM * 2, 0x00020000);
  const unsigned base0 = (unsigned)(b * NH + h0) * (SK * DH * 2), base1 = (unsigned)(b * NH + h1) * (SK * DH * 2);
  unsigned vkv = ((lane >> 2) & 3) * 256 + (lane >> 4) * 64 + (lane & 3) * 16;
  const unsigned obase = (unsigned)(blk * NB + b) * (SQ * 8192);
  u64* const flg = (u64*)(ws + WS_F);
  u64* const oflag = flg + F_O / 8 + ((blk * NB + b) * 4 + hg) * 8;
  const int tile = (blk * NB + b) * 4 + nq;
  u64* const xflag = flg + F_X / 8 + tile * 16;
  GRd gr;
  grd_init(gr, w, lane, L_SX, L_SW);
  f32x4 acc[2][9];
  acc_zero(acc);
  f32x16 o[4]; float l = 0.f;
  Ninth n;
  if (g == 0) {
    const u64 gen = ld_flag(flg + F_GEN / 8);
    {
      const rsrc_t rC = __builtin_amdgcn_make_buffer_rsrc((void*)pc, 0, len, 0x00020000);
      dma<0, 0>(162816, lane * 16, rC, ((jj & 15) * 4 + w) * 1024);
    }
    BARRIER();
    KvPre kvp; kvp.rK = rK; kvp.rV = rV; kvp.base = base0; kvp.vkv = vkv; kvp.wq = w * 4096;
    gemm16<KVIT_>(gr, gr, acc, &kvp);
    PIN(lane);
    q_write(acc, w, lane, L_QB0, L_Q9);
    if (wv == 0) {
      const u64 tg = gen + 1;
      *(LDS3 u32x2*)L_TAG = u32x2{(unsigned)tg, (unsigned)(tg >> 32)};
      st_flag(flg + F_START / 8 + bid, tg);
    }
    BARRIER();
    WAITV(8);
    __builtin_amdgcn_s_setprio(MPRIO);
    attention<false>(lane, w, L_QB0 + w * 8192 + lane * 16, o, l, rK, rV, base0, base1, vkv);
    __builtin_amdgcn_s_setprio(0);
    PIN(lane);
    const u64 tag = get_tag();
    grd_init(gr, w, lane, L_SX, L_SW);
    Duty d;
    duty_init(d, w, lane, rO, obase + hA * 256, 8192, rWo, hA * 128 * 1024 + nq * 256, 1024, 4 * nq);
    bf16x8 wb[2];
    ninth_init(n, w, lane, L_Q9 + 4096);
    hintA<0, 1, false, 0, -1>(n, 1 * 16384, gr, acc, wb, d, 0);
    const u32x4 r9 = merge9(w, lane, L_PB);
    WAITV(4);
    BARRIER();
    store9(w, lane, r9, rO, obase + h0 * 256, L_SX);
    epilogue1(w, lane, o, l, rO, obase + h0 * 256, L_SX);
#pragma unroll
    for (int st = 0; st < 4; ++st) duty_w<2>(d, st, false);
    hintB<0, 1, false, 0, 0, -1>(n, 2 * 16384, gr, acc, wb, d, 0);
    WAITV(8);
    BARRIER();
    if (w == 0) st_flag(oflag + nq, tag);
    acc_zero(acc);
    hintA<0, 2, false, 0, -1>(n, 3 * 16384, gr, acc, wb, d, 0);
    WAITV(0);
    BARRIER();
    hintB<0, 2, true, 0, SPLITM, -1>(n, 4 * 16384, gr, acc, wb, d, 0);
    poll_flags(oflag, 4, nq, tag, lane);
    phase2<3>(w, lane, n, gr, acc, wb, d);
    PIN(lane);
    GRd ga, gb;
    grd_init(ga, w, lane, L_AX, L_SW);
    grd_init(gb, w, lane, L_SX, L_AW);
    BARRIER();
    gemm16<-1>(ga, gb, acc);
    PIN(lane);
    if (w == hg) {
#pragma unroll
      for (int t = 0; t < 2; ++t)
#pragma unroll
        for (int m = 0; m < 9; ++m) *(LDS3 f32x4*)(L_FIN + (t * 9 + m) * 1024 + lane * 16) = acc[t][m];
    } else {
      const rsrc_t rX = __builtin_amdgcn_make_buffer_rsrc((void*)(ws + WS_X), 0, 64 * 16 * 18432, 0x00020000);
      const unsigned so = (unsigned)((tile * 4 + w) * 4 + hg) * 18432;
#pragma unroll
      for (int t = 0; t < 2; ++t)
#pragma unroll
        for (int m = 0; m < 9; ++m) __builtin_amdgcn_raw_buffer_store_b128(__builtin_bit_cast(u32x4, acc[t][m]), rX, so + (t * 9 + m) * 1024 + lane * 16, 0, 16);
      WAITV(0);
      st_flag(xflag + w * 4 + hg, tag);
    }
  } else {
    Duty d;
    {
      const rsrc_t rX = __builtin_amdgcn_make_buffer_rsrc((void*)(blk ? xB : xA), 0, NB * SQ * DM * 2, 0x00020000);
      const rsrc_t rWq = __builtin_amdgcn_make_buffer_rsrc((void*)(blk ? wqB : wqA), 0, DM * NH * DH * 2, 0x00020000);
      duty_init(d, w, lane, rX, b * (SQ * DM * 2), 1024, rWq, h0 * 256, 8192, QROT);
    }
    p0_loader<3>(d, (unsigned)(h1 - h0) * 256u);
    BARRIER();
    phase1<3>(w, lane, gr, d);
    __builtin_amdgcn_s_setprio(MPRIO);
    attention<true>(lane, w, L_PA + w * 8192 + lane * 16, o, l, rK, rV, base1, base1, vkv);
    __builtin_amdgcn_s_setprio(0);
    PIN(lane);
    const u64 tag = get_tag();
    duty_init(d, w, lane, rO, obase + (hA ^ 4) * 256, 8192, rWo, (hA ^ 4) * 128 * 1024 + nq * 256, 1024, 4 * nq);
    duty_w<2>(d, 0, false);
    epilogue1(w, lane, o, l, rO, obase + h1 * 256, L_AX);
    BARRIER();
    duty_w<2>(d, 1, false);
    store9(w, lane, merge9(w, lane, L_QC), rO, obase + h1 * 256, L_AX);
    WAITV(0);
    BARRIER();
    if (w == 0) st_flag(oflag + 4 + nq, tag);
    tail_loader<3>(lane, d, oflag + 4, nq, tag);
    if (w == 0) poll_flags(xflag + hg * 4, 4, hg, tag, lane);
    if (bid == 0 && w == 1) {
      const u64* sp = flg + F_START / 8 + lane;
      for (int it = 0; it < (1 << 16); ++it) {
        const u64 a0 = ld_flag(sp), a1 = ld_flag(sp + 64), a2 = ld_flag(sp + 128), a3 = ld_flag(sp + 192);
        if (__all(a0 == tag && a1 == tag && a2 == tag && a3 == tag)) break;
        __builtin_amdgcn_s_sleep(2);
      }
      st_flag(flg + F_GEN / 8, tag);
    }
  }
  BARRIER();
  PIN(lane);
  {
    const rsrc_t rX = __builtin_amdgcn_make_buffer_rsrc((void*)(ws + WS_X), 0, 64 * 16 * 18432, 0x00020000);
    const unsigned so = (unsigned)((tile * 4 + hg) * 4) * 18432;
    const rsrc_t rOut = __builtin_amdgcn_make_buffer_rsrc((void*)out, 0, NB * SQ * 1024 * 2, 0x00020000);
    const unsigned ob = ((b * SQ + (lane & 15)) * 1024 + blk * 512 + nq * 128 + 32 * hg + 4 * (lane >> 4)) * 2;
#pragma unroll
    for (int r = 0; r < 3; ++r) {
      const int ti = wv + 8 * r;
      if (ti < 18) {
        f32x4 v = *(LDS3 f32x4*)(L_FIN + ti * 1024 + lane * 16);
#pragma unroll
        for (int sh = 1; sh < 4; ++sh) {
          const int src = (hg + sh) & 3;
          v += __builtin_bit_cast(f32x4, __builtin_amdgcn_raw_buffer_load_b128(rX, ti * 1024 + lane * 16, so + src * 18432, 16));
        }
        const int t = ti >= 9 ? 1 : 0, m = ti - 9 * t;
        __builtin_amdgcn_raw_buffer_store_b64(u32x2{cvtpk(v[0], v[1]), cvtpk(v[2], v[3])}, rOut, ob + m * 16 * 2048 + t * 32, 0, 2);
      }
    }
  }
  asm volatile("kf_code_end:");
}

extern "C" void kernel_launch(void* const* d_in, const int* in_sizes, int n_in, void* d_out, int out_size, void* d_ws, size_t ws_size, hipStream_t stream) {
  (void)hipFuncSetAttribute((const void*)k_fused, hipFuncAttributeMaxDynamicSharedMemorySize, LDS_BYTES);
  k_fused<<<256, 512, LDS_BYTES, stream>>>((const char*)d_in[0], (const char*)d_in[1], (const char*)d_in[2], (const char*)d_in[3], (const char*)d_in[4],
                                           (const char*)d_in[5], (const char*)d_in[6], (const char*)d_in[7], (const char*)d_in[8], (const char*)d_in[9],
                                           (char*)d_ws, (char*)d_out);
}
```
